```python
import torch

def _softmax(x, axis=-1):
    return torch.softmax(x, dim=axis)

def _gelu(x):
    return torch.nn.functional.gelu(x, approximate='tanh')

def _silu(x):
    return torch.nn.functional.silu(x)

def _sigmoid(x):
    return torch.sigmoid(x)

def _layernorm(x, axis=-1, eps=1e-05):
    m = x.mean(dim=axis, keepdim=True)
    v = x.var(dim=axis, keepdim=True, unbiased=False)
    return (x - m) / torch.sqrt(v + eps)

def _scatter_add(tgt, idx, src, axis=0):
    return tgt.clone().index_add_(axis, idx, src)

def _segment_sum(src, seg, n):
    out = torch.zeros((n,) + src.shape[1:], dtype=src.dtype)
    return out.index_add_(0, seg, src)

def _pad(x, pw):
    flat = tuple((v for pair in reversed(pw) for v in pair))
    return torch.nn.functional.pad(x, flat)
kernel_name = 'standalone__conformer_family_zipformer__speech'

def setup_inputs():

    _w = lambda a: (lambda t: t.to(torch.bfloat16) if t.is_floating_point() else t)(torch.as_tensor(a))
    B, T, D, K, H = (2, 1000, 256, 31, 4)
    sc = 0.1
    return {'x': _w((lambda *s: torch.randn(*s, dtype=torch.bfloat16) * 0.8)(B, T, D)), 'w_ff1a': _w((lambda *s: torch.randn(*s, dtype=torch.bfloat16) * 0.8)(D, 4 * D) * sc), 'w_ff1b': _w((lambda *s: torch.randn(*s, dtype=torch.bfloat16) * 0.8)(4 * D, D) * sc), 'w_qkv': _w((lambda *s: torch.randn(*s, dtype=torch.bfloat16) * 0.8)(D, 3 * D) * sc), 'w_o': _w((lambda *s: torch.randn(*s, dtype=torch.bfloat16) * 1.6)(D, D) * sc), 'w_pw1': _w((lambda *s: torch.randn(*s, dtype=torch.bfloat16) * 1.6)(D, 2 * D) * sc), 'dw': _w((lambda *s: torch.randn(*s, dtype=torch.bfloat16) * 0.4)(K, D)), 'w_pw2': _w((lambda *s: torch.randn(*s, dtype=torch.bfloat16) * 0.4)(D, D) * sc), 'w_ff2a': _w((lambda *s: torch.randn(*s, dtype=torch.bfloat16) * 0.8)(D, 4 * D) * sc), 'w_ff2b': _w((lambda *s: torch.randn(*s, dtype=torch.bfloat16) * 0.8)(4 * D, D) * sc)}

def reference(x, w_ff1a, w_ff1b, w_qkv, w_o, w_pw1, dw, w_pw2, w_ff2a, w_ff2b):
    B, T, D = x.shape
    K = dw.shape[0]
    H = 4
    Dh = D // H
    h = _layernorm(x, axis=-1)
    h = _silu(h @ w_ff1a) @ w_ff1b
    x = x + 0.5 * h
    h = _layernorm(x, axis=-1)
    qkv = h @ w_qkv
    q = (lambda x, *d: x.permute(*d))(qkv[..., :D].reshape(B, T, H, Dh), 0, 2, 1, 3)
    k = (lambda x, *d: x.permute(*d))(qkv[..., D:2 * D].reshape(B, T, H, Dh), 0, 2, 1, 3)
    v = (lambda x, *d: x.permute(*d))(qkv[..., 2 * D:].reshape(B, T, H, Dh), 0, 2, 1, 3)
    scale = Dh ** (-0.5)
    scores = torch.einsum('bhqd,bhkd->bhqk', q, k) * scale
    p = _softmax(scores, axis=-1)
    att = torch.einsum('bhqk,bhkd->bhqd', p, v)
    att = (lambda x, *d: x.permute(*d))(att, 0, 2, 1, 3).reshape(B, T, D)
    x = x + att @ w_o
    h = _layernorm(x, axis=-1)
    h = h @ w_pw1
    a, b = (h[..., :D].clone(), h[..., D:].clone())
    h = a * _sigmoid(b)
    pad = K // 2
    hp = _pad(h, ((0, 0), (pad, pad), (0, 0)))
    c = torch.zeros_like(h)
    for i in range(K):
        c = c + hp[:, i:i + T, :] * dw[i][None, None, :]
    x = x + _silu(c) @ w_pw2
    h = _layernorm(x, axis=-1)
    h = _silu(h @ w_ff2a) @ w_ff2b
    x = x + 0.5 * h
    return _layernorm(x, axis=-1)
```

```cpp
#include <hip/hip_runtime.h>
#include <hip/hip_bf16.h>
typedef unsigned short u16;
__device__ inline float b2f(u16 v){ return __uint_as_float(((unsigned)v)<<16); }
__device__ inline u16 f2b(float f){ unsigned u=__float_as_uint(f); u += 0x7fffu + ((u>>16)&1u); return (u16)(u>>16); }
constexpr int B=2,T=1000,D=256,KC=31,H=4,DH=64,M=B*T;

__global__ void k_cvt(const u16* x, float* y, int n){ int i=blockIdx.x*256+threadIdx.x; if(i<n) y[i]=b2f(x[i]); }
__global__ void k_ln(const float* x, float* y, u16* yb){
  int r=blockIdx.x, t=threadIdx.x; __shared__ float s[256];
  float v=x[r*256+t]; s[t]=v; __syncthreads();
  for(int o=128;o>0;o>>=1){ if(t<o) s[t]+=s[t+o]; __syncthreads(); }
  float m=s[0]/256; __syncthreads();
  float d=v-m; s[t]=d*d; __syncthreads();
  for(int o=128;o>0;o>>=1){ if(t<o) s[t]+=s[t+o]; __syncthreads(); }
  float var=s[0]/256;
  float o=d*rsqrtf(var+1e-5f);
  if(y) y[r*256+t]=b2f(f2b(o));
  if(yb) yb[r*256+t]=f2b(o);
}
__global__ void k_gemm(const float* A, const u16* W, float* C, const float* res, int N, int K, int mode, float alpha){
  int r=blockIdx.y; int c=blockIdx.x*256+threadIdx.x;
  float acc=0; for(int k=0;k<K;k++) acc+=A[r*K+k]*b2f(W[k*N+c]);
  if(mode==1){ acc=acc/(1.f+__expf(-acc)); acc=b2f(f2b(acc)); }
  acc*=alpha; if(res) acc+=res[r*N+c];
  if(mode==2) acc=b2f(f2b(acc));
  C[r*N+c]=acc;
}
__global__ void k_attn(const float* qkv, float* att){
  int q=blockIdx.x, h=blockIdx.y, b=blockIdx.z, t=threadIdx.x;
  __shared__ float s[1024]; __shared__ float red[256]; __shared__ float qv[64];
  const float* base=qkv+(size_t)b*T*768;
  if(t<64) qv[t]=base[q*768+h*64+t];
  __syncthreads();
  float mx=-1e30f;
  for(int k=t;k<T;k+=256){ float a=0; for(int d=0;d<64;d++) a+=qv[d]*base[k*768+256+h*64+d]; a*=0.125f; s[k]=a; mx=fmaxf(mx,a);}
  red[t]=mx; __syncthreads();
  for(int o=128;o>0;o>>=1){ if(t<o) red[t]=fmaxf(red[t],red[t+o]); __syncthreads(); }
  mx=red[0]; __syncthreads();
  float sum=0; for(int k=t;k<T;k+=256){ float e=__expf(s[k]-mx); s[k]=e; sum+=e; }
  red[t]=sum; __syncthreads();
  for(int o=128;o>0;o>>=1){ if(t<o) red[t]+=red[t+o]; __syncthreads(); }
  sum=red[0];
  if(t<64){ float a=0; for(int k=0;k<T;k++) a+=s[k]*base[k*768+512+h*64+t]; att[(b*T+q)*256+h*64+t]=b2f(f2b(a/sum)); }
}
__global__ void k_glu(const float* h, float* g){ int r=blockIdx.x,t=threadIdx.x; float a=h[r*512+t], b=h[r*512+256+t]; g[r*256+t]=b2f(f2b(a/(1.f+__expf(-b)))); }
__global__ void k_conv(const float* g, const u16* dw, float* c){
  int r=blockIdx.x,t=threadIdx.x; int b=r/T, tt=r%T; float a=0;
  for(int i=0;i<KC;i++){ int s=tt+i-15; if(s>=0&&s<T) a+=g[(b*T+s)*256+t]*b2f(dw[i*256+t]); }
  a=a/(1.f+__expf(-a)); c[r*256+t]=b2f(f2b(a));
}
extern "C" void kernel_launch(void* const* d_in, const int* in_sizes, int n_in, void* d_out, int out_size, void* d_ws, size_t ws_size, hipStream_t stream){
  const u16* x=(const u16*)d_in[0]; const u16* w1a=(const u16*)d_in[1]; const u16* w1b=(const u16*)d_in[2]; const u16* wqkv=(const u16*)d_in[3];
  const u16* wo=(const u16*)d_in[4]; const u16* wp1=(const u16*)d_in[5]; const u16* dw=(const u16*)d_in[6]; const u16* wp2=(const u16*)d_in[7];
  const u16* w2a=(const u16*)d_in[8]; const u16* w2b=(const u16*)d_in[9];
  float* ws=(float*)d_ws; float* xf=ws; float* h=xf+M*256; float* big=h+M*256; float* x1=big+M*1024; float* att=x1+M*256; float* x2=att+M*256; float* g=x2+M*256; float* c=g+M*256; float* x3=c+M*256; float* x4=x3+M*256;
  k_cvt<<<M,256,0,stream>>>(x,xf,M*256);
  k_ln<<<M,256,0,stream>>>(xf,h,nullptr);
  k_gemm<<<dim3(4,M),256,0,stream>>>(h,w1a,big,nullptr,1024,256,1,1.f);
  k_gemm<<<dim3(1,M),256,0,stream>>>(big,w1b,x1,xf,256,1024,0,0.5f);
  k_ln<<<M,256,0,stream>>>(x1,h,nullptr);
  k_gemm<<<dim3(3,M),256,0,stream>>>(h,wqkv,big,nullptr,768,256,2,1.f);
  k_attn<<<dim3(T,H,B),256,0,stream>>>(big,att);
  k_gemm<<<dim3(1,M),256,0,stream>>>(att,wo,x2,x1,256,256,0,1.f);
  k_ln<<<M,256,0,stream>>>(x2,h,nullptr);
  k_gemm<<<dim3(2,M),256,0,stream>>>(h,wp1,big,nullptr,512,256,0,1.f);
  k_glu<<<M,256,0,stream>>>(big,g);
  k_conv<<<M,256,0,stream>>>(g,dw,c);
  k_gemm<<<dim3(1,M),256,0,stream>>>(c,wp2,x3,x2,256,256,0,1.f);
  k_ln<<<M,256,0,stream>>>(x3,h,nullptr);
  k_gemm<<<dim3(4,M),256,0,stream>>>(h,w2a,big,nullptr,1024,256,1,1.f);
  k_gemm<<<dim3(1,M),256,0,stream>>>(big,w2b,x4,x3,256,1024,0,0.5f);
  k_ln<<<M,256,0,stream>>>(x4,nullptr,(u16*)d_out);
}
```

```cpp
#include <hip/hip_runtime.h>
#include <hip/hip_bf16.h>

typedef unsigned short u16;
typedef unsigned long long u64;
typedef __attribute__((ext_vector_type(2))) unsigned u32x2;
typedef __attribute__((ext_vector_type(4))) unsigned u32x4;
typedef __attribute__((ext_vector_type(2))) float f32x2;
typedef __attribute__((ext_vector_type(4))) float f32x4;
typedef __attribute__((ext_vector_type(16))) float f32x16;
typedef __attribute__((ext_vector_type(2))) __bf16 bf16x2;
typedef __attribute__((ext_vector_type(4))) __bf16 bf16x4;
typedef __attribute__((ext_vector_type(8))) __bf16 bf16x8;
typedef __attribute__((ext_vector_type(4))) short s16x4;
typedef __amdgpu_buffer_rsrc_t rsrc_t;
#define DI __device__ __forceinline__
#define LAS __attribute__((address_space(3)))
#define SB() __builtin_amdgcn_sched_barrier(0)

constexpr int T_LEN = 1000;
constexpr float LOG2E = 1.4426950408889634f;

constexpr unsigned WS_P1  = 0;
constexpr unsigned WS_KV  = WS_P1 + (4u << 20);
constexpr unsigned WS_Q   = WS_KV + (2u << 20);
constexpr unsigned WS_ATT = WS_Q + (1u << 20);
constexpr unsigned WS_G   = WS_ATT + (1u << 20);
constexpr unsigned WS_X   = WS_G + (1u << 20);
constexpr unsigned WS_P2  = WS_X + (3u << 20);
constexpr unsigned WS_WT  = WS_P2 + (4u << 20);
constexpr unsigned WT_WO = 0, WT_P2 = 128u << 10, WT_2A = 256u << 10, WT_2B = 768u << 10, WT_P1 = 1280u << 10, WT_DW = 1536u << 10;
constexpr unsigned WS_END = WS_WT + (2u << 20);
constexpr unsigned WS_PROF = 64u << 20;

constexpr int L_ACT0 = 0, L_ACT1 = 16384, L_STAT = 32768, L_Q = 36864, L_COMB = 40960, L_ML = L_COMB + 65536, L_END = L_ML + 2048;
constexpr int G_ROW = 144;

DI rsrc_t mk_rsrc(const void* p, unsigned bytes) { return __builtin_amdgcn_make_buffer_rsrc((void*)p, 0, bytes, 0x00020000); }
DI unsigned pack2(float a, float b) { bf16x2 v = {(__bf16)a, (__bf16)b}; return __builtin_bit_cast(unsigned, v); }
DI float lo16(unsigned u) { return __uint_as_float(u << 16); }
DI float hi16(unsigned u) { return __uint_as_float(u & 0xffff0000u); }
DI f32x4 mfma16(u32x4 a, u32x4 b, f32x4 c) { return __builtin_amdgcn_mfma_f32_16x16x32_bf16(__builtin_bit_cast(bf16x8, a), __builtin_bit_cast(bf16x8, b), c, 0, 0, 0); }
DI f32x16 mfma32(u32x4 a, u32x4 b, f32x16 c) { return __builtin_amdgcn_mfma_f32_32x32x16_bf16(__builtin_bit_cast(bf16x8, a), __builtin_bit_cast(bf16x8, b), c, 0, 0, 0); }
DI f32x4 mfma4(u32x2 a, u32x2 b, f32x4 c) { return __builtin_amdgcn_mfma_f32_4x4x4bf16_1k(__builtin_bit_cast(s16x4, a), __builtin_bit_cast(s16x4, b), c, 0, 0, 0); }
DI float silu_f(float x) { return x * __builtin_amdgcn_rcpf(1.f + __builtin_amdgcn_exp2f(-LOG2E * x)); }
DI float sigm_f(float x) { return __builtin_amdgcn_rcpf(1.f + __builtin_amdgcn_exp2f(-LOG2E * x)); }
DI u32x4 lds_r128(char* sm, int off) { return *(u32x4*)(sm + off); }
DI void lds_w128(char* sm, int off, u32x4 v) { *(u32x4*)(sm + off) = v; }
DI float swadd32(float a, float b) { auto r = __builtin_amdgcn_permlane32_swap(__float_as_uint(a), __float_as_uint(b), false, false); return __uint_as_float(r[0]) + __uint_as_float(r[1]); }
DI float swadd16(float a, float b) { auto r = __builtin_amdgcn_permlane16_swap(__float_as_uint(a), __float_as_uint(b), false, false); return __uint_as_float(r[0]) + __uint_as_float(r[1]); }

struct Ctx { int tid, w, lane, tok, g, b, tile, s, tg; };
DI Ctx mk_ctx() {
  Ctx c; c.tid = threadIdx.x; c.w = __builtin_amdgcn_readfirstlane(threadIdx.x >> 6); c.lane = c.tid & 63; c.tok = c.lane & 15; c.g = c.lane >> 4;
  int bid = blockIdx.x; c.s = (bid >> 3) & 3; c.tg = (bid & 7) * 8 + (bid >> 5); c.b = c.tg >> 5; c.tile = c.tg & 31; return c;
}

template <int LD> DI void load_raw(unsigned (&raw)[64], rsrc_t rs, int voff) {
#pragma unroll
  for (int st = 0; st < 8; st++)
#pragma unroll
    for (int j = 0; j < 8; j++) raw[st * 8 + j] = __builtin_amdgcn_raw_buffer_load_b32(rs, voff, (st * 32 + j) * LD, 0);
}
DI u32x4 tr_frag(const unsigned* r8, int i) {
  const unsigned sel = i ? 0x07060302u : 0x05040100u;
  u32x4 f;
#pragma unroll
  for (int m = 0; m < 4; m++) f[m] = __builtin_amdgcn_perm(r8[2 * m + 1], r8[2 * m], sel);
  return f;
}
DI void load_tiled(u32x4 (&fr)[16], rsrc_t rs, int voff, int soff) {
#pragma unroll
  for (int q = 0; q < 16; q++) fr[q] = __builtin_amdgcn_raw_buffer_load_b128(rs, voff + (q & 3) * 1024, soff + (q >> 2) * 4096, 0);
}
template <bool SWAP> DI void gemm_raw(f32x4 (&acc)[2][2], const unsigned (&raw)[64], char* sm, int act, const Ctx& c) {
  const int base = act + (c.g * 32 + c.tok) * 16;
#pragma unroll
  for (int st = 0; st < 8; st++) {
    u32x4 b0 = lds_r128(sm, base + st * 2048), b1 = lds_r128(sm, base + st * 2048 + 256);
#pragma unroll
    for (int i = 0; i < 2; i++) {
      u32x4 a = tr_frag(&raw[st * 8], i);
      acc[i][0] = SWAP ? mfma16(b0, a, acc[i][0]) : mfma16(a, b0, acc[i][0]);
      acc[i][1] = SWAP ? mfma16(b1, a, acc[i][1]) : mfma16(a, b1, acc[i][1]);
    }
  }
}
DI void gemm_tiled(f32x4 (&acc)[2][2], const u32x4 (&fr)[16], char* sm, int act, const Ctx& c) {
  const int base = act + (c.g * 32 + c.tok) * 16;
#pragma unroll
  for (int st = 0; st < 8; st++) {
    u32x4 b0 = lds_r128(sm, base + st * 2048), b1 = lds_r128(sm, base + st * 2048 + 256);
#pragma unroll
    for (int i = 0; i < 2; i++) {
      acc[i][0] = mfma16(fr[st * 2 + i], b0, acc[i][0]);
      acc[i][1] = mfma16(fr[st * 2 + i], b1, acc[i][1]);
    }
  }
}
DI void zero_acc(f32x4 (&acc)[2][2]) {
#pragma unroll
  for (int i = 0; i < 2; i++)
#pragma unroll
    for (int t = 0; t < 2; t++) acc[i][t] = f32x4{0.f, 0.f, 0.f, 0.f};
}
DI void unpack8(float (&v)[8], u32x4 p) {
#pragma unroll
  for (int m = 0; m < 4; m++) { v[2 * m] = lo16(p[m]); v[2 * m + 1] = hi16(p[m]); }
}
DI u32x4 pack8(const float (&v)[8]) { return u32x4{pack2(v[0], v[1]), pack2(v[2], v[3]), pack2(v[4], v[5]), pack2(v[6], v[7])}; }
DI void act_store(char* sm, int act, const Ctx& c, u32x4 p0, u32x4 p1) {
  const int o = act + ((c.w * 4 + c.g) * 32 + c.tok) * 16;
  lds_w128(sm, o, p0); lds_w128(sm, o + 256, p1);
}
DI void ln_to_act(const u32x4 (&xr)[2], char* sm, int act, int stat, const Ctx& c) {
  float v[2][8]; unpack8(v[0], xr[0]); unpack8(v[1], xr[1]);
  float s0 = 0, s1 = 0, q0 = 0, q1 = 0;
#pragma unroll
  for (int e = 0; e < 8; e++) { s0 += v[0][e]; s1 += v[1][e]; q0 = fmaf(v[0][e], v[0][e], q0); q1 = fmaf(v[1][e], v[1][e], q1); }
  float t = swadd16(swadd32(s0, s1), swadd32(q0, q1));
  *(float*)(sm + stat + (c.w * 64 + c.lane) * 4) = t;
  __syncthreads();
  float u = 0;
#pragma unroll
  for (int k = 0; k < 8; k++) u += *(float*)(sm + stat + (k * 64 + c.lane) * 4);
  auto r = __builtin_amdgcn_permlane16_swap(__float_as_uint(u), __float_as_uint(u), false, false);
  float mean = __uint_as_float(r[0]) * (1.f / 256.f);
  float var = fmaxf(fmaf(-mean, mean, __uint_as_float(r[1]) * (1.f / 256.f)), 0.f);
  float rstd = __builtin_amdgcn_rsqf(var + 1e-5f), nm = -mean * rstd;
  auto rr = __builtin_amdgcn_permlane32_swap(__float_as_uint(rstd), __float_as_uint(rstd), false, false);
  auto rn = __builtin_amdgcn_permlane32_swap(__float_as_uint(nm), __float_as_uint(nm), false, false);
#pragma unroll
  for (int t2 = 0; t2 < 2; t2++)
#pragma unroll
    for (int e = 0; e < 8; e++) v[t2][e] = fmaf(v[t2][e], __uint_as_float(rr[t2]), __uint_as_float(rn[t2]));
  act_store(sm, act, c, pack8(v[0]), pack8(v[1]));
  __syncthreads();
}
DI void acc_row(float (&v)[8], const f32x4 (&acc)[2][2], int th) {
#pragma unroll
  for (int j = 0; j < 4; j++) { v[2 * j] = acc[0][th][j]; v[2 * j + 1] = acc[1][th][j]; }
}
DI int chmap(int slot) { return (slot & 0xC0) + ((slot >> 4) & 3) * 16 + (slot & 7) * 2 + ((slot >> 3) & 1); }

struct Params { const u16 *x, *w1a, *w1b, *wqkv, *wo, *wp1, *dw, *wp2, *w2a, *w2b; u16* out; char* ws; };

template <int AUX> DI void prep_task(const Params& p, int task, rsrc_t rws, unsigned wt, int lane) {
  const int r = lane & 15, g = lane >> 4;
  if (task < 768) {
    const u16* src; int ld, rowbase, jst, col; unsigned d0, d1; unsigned bytes;
    const int st = task & 7;
    if (task < 64) { const int u = task >> 3, C = 4 * st + g, quad = C & 3; src = p.wo; ld = 512; bytes = 131072; rowbase = 32 * (C >> 2) + 16 * (quad & 1) + (quad >> 1); jst = 2; col = 32 * u + 2 * r; d0 = WT_WO + (task * 2) * 1024; d1 = d0 + 1024; }
    else if (task < 128) { const int t = task - 64, u = t >> 3, w = 4 * (st & 1) + g; src = p.wp2; ld = 512; bytes = 131072; rowbase = 64 * (st >> 1) + 16 * (w >> 1) + (w & 1); jst = 2; col = 32 * u + 2 * r; d0 = WT_P2 + (t * 2) * 1024; d1 = d0 + 1024; }
    else if (task < 384) { const int t = task - 128, su = t >> 3; src = p.w2a; ld = 2048; bytes = 524288; rowbase = 32 * st + 8 * g; jst = 1; col = 32 * su + 2 * r; d0 = WT_2A + (t * 2) * 1024; d1 = d0 + 1024; }
    else if (task < 640) { const int t = task - 384, su = t >> 3; src = p.w2b; ld = 512; bytes = 524288; rowbase = 256 * (su >> 3) + 32 * st + 8 * g; jst = 1; col = 32 * (su & 7) + 2 * r; d0 = WT_2B + (t * 2) * 1024; d1 = d0 + 1024; }
    else { const int t = task - 640, v = t >> 3; src = p.wp1; ld = 1024; bytes = 262144; rowbase = 32 * st + 8 * g; jst = 1; col = 16 * v + 2 * (r & 7) + (r >> 3) * 256; d0 = WT_P1 + ((2 * v) * 8 + st) * 1024; d1 = d0 + 8192; }
    rsrc_t rs = mk_rsrc(src, bytes);
    unsigned raw[8];
#pragma unroll
    for (int j = 0; j < 8; j++) raw[j] = __builtin_amdgcn_raw_buffer_load_b32(rs, rowbase * ld + col * 2, j * jst * ld, 0);
    __builtin_amdgcn_raw_buffer_store_b128(tr_frag(raw, 0), rws, lane * 16, wt + d0, AUX);
    __builtin_amdgcn_raw_buffer_store_b128(tr_frag(raw, 1), rws, lane * 16, wt + d1, AUX);
  } else if (task < 848) {
    const int t = task - 768, grp = t / 5, m = t - grp * 5;
    const int ch = chmap(16 * grp + (lane >> 2)), i = lane & 3;
    rsrc_t rs = mk_rsrc(p.dw, 31 * 512);
    unsigned h[8];
#pragma unroll
    for (int e = 0; e < 8; e++) { int tap = 8 * m + e - 1 - i; h[e] = __builtin_amdgcn_raw_buffer_load_b16(rs, (unsigned)tap < 31u ? tap * 512 + ch * 2 : 0x100000, 0, 0); }
    u32x4 o = {h[0] | (h[1] << 16), h[2] | (h[3] << 16), h[4] | (h[5] << 16), h[6] | (h[7] << 16)};
    __builtin_amdgcn_raw_buffer_store_b128(o, rws, lane * 16, wt + WT_DW + t * 1024, AUX);
  }
}

DI void load_x(u32x4 (&xr)[2], const Params& p, const Ctx& c) {
  rsrc_t rx = mk_rsrc(p.x, 2000 * 512);
#pragma unroll
  for (int th = 0; th < 2; th++) {
    int t = c.tile * 32 + th * 16 + c.tok;
    int off = t < T_LEN ? (c.b * T_LEN + t) * 512 + (c.w * 32 + c.g * 8) * 2 : 0x10000000;
    xr[th] = __builtin_amdgcn_raw_buffer_load_b128(rx, off, 0, 0);
  }
}
#define STAMP(k, i)
#define KATTR __global__ __launch_bounds__(512) __attribute__((amdgpu_waves_per_eu(2, 2)))

KATTR void k1(Params p) {
  __shared__ __attribute__((aligned(16))) char sm[L_Q];
  STAMP(0, 0);
  const Ctx c = mk_ctx();
  rsrc_t rws = mk_rsrc(p.ws, WS_END);
  u32x4 xr[2]; load_x(xr, p, c);
  unsigned wa[64], wb[64];
  load_raw<2048>(wa, mk_rsrc(p.w1a, 524288), c.g * 8 * 2048 + (c.s * 256 + c.w * 32 + c.tok * 2) * 2);
  SB();
  ln_to_act(xr, sm, L_ACT0, L_STAT, c);
  SB();
  load_raw<512>(wb, mk_rsrc(p.w1b, 524288), (c.s * 256 + c.g * 8) * 512 + (c.w * 32 + c.tok * 2) * 2);
  SB();
  f32x4 acc[2][2]; zero_acc(acc);
  gemm_raw<false>(acc, wa, sm, L_ACT0, c);
  float v[2][8];
#pragma unroll
  for (int th = 0; th < 2; th++) { acc_row(v[th], acc, th);
#pragma unroll
    for (int e = 0; e < 8; e++) v[th][e] = silu_f(v[th][e]); }
  act_store(sm, L_ACT1, c, pack8(v[0]), pack8(v[1]));
  __syncthreads();
  zero_acc(acc);
  gemm_raw<false>(acc, wb, sm, L_ACT1, c);
#pragma unroll
  for (int th = 0; th < 2; th++) { acc_row(v[th], acc, th);
    __builtin_amdgcn_raw_buffer_store_b128(pack8(v[th]), rws, c.tid * 16 + th * 8192, WS_P1 + (c.tg * 4 + c.s) * 16384, 0); }
  if (c.w < 4) prep_task<0>(p, c.w * 256 + blockIdx.x, rws, WS_WT, c.lane);
  STAMP(0, 1);
}

KATTR void k2(Params p) {
  __shared__ __attribute__((aligned(16))) char sm[L_Q];
  STAMP(1, 0);
  const Ctx c = mk_ctx();
  rsrc_t rws = mk_rsrc(p.ws, WS_END);
  u32x4 xr[2]; load_x(xr, p, c);
  u32x4 pp[4][2];
#pragma unroll
  for (int s = 0; s < 4; s++)
#pragma unroll
    for (int th = 0; th < 2; th++) pp[s][th] = __builtin_amdgcn_raw_buffer_load_b128(rws, c.tid * 16 + th * 8192, WS_P1 + (c.tg * 4 + s) * 16384, 0);
  unsigned wq[64];
  if (c.w < 6) load_raw<1536>(wq, mk_rsrc(p.wqkv, 393216), c.g * 8 * 1536 + ((c.w >> 1) * 256 + c.s * 64 + (c.w & 1) * 32 + c.tok * 2) * 2);
  SB();
#pragma unroll
  for (int th = 0; th < 2; th++) {
    float x[8], a[8], t[8]; unpack8(x, xr[th]); unpack8(a, pp[0][th]);
#pragma unroll
    for (int s = 1; s < 4; s++) { unpack8(t, pp[s][th]);
#pragma unroll
      for (int e = 0; e < 8; e++) a[e] += t[e]; }
#pragma unroll
    for (int e = 0; e < 8; e++) x[e] = fmaf(a[e], 0.5f, x[e]);
    xr[th] = pack8(x);
    if (c.s == 0) __builtin_amdgcn_raw_buffer_store_b128(xr[th], rws, c.tid * 16 + th * 8192, WS_X + c.tg * 16384, 0);
  }
  ln_to_act(xr, sm, L_ACT0, L_STAT, c);
  f32x4 acc[2][2]; zero_acc(acc);
  const unsigned kvb = WS_KV + ((c.b * 4 + c.s) * 32 + c.tile) * 8192;
  if (c.w < 4) {
    gemm_raw<false>(acc, wq, sm, L_ACT0, c);
    const unsigned dst = c.w < 2 ? WS_Q + (c.tg * 4 + c.s) * 4096 : kvb;
#pragma unroll
    for (int th = 0; th < 2; th++) { float v[8]; acc_row(v, acc, th);
      __builtin_amdgcn_raw_buffer_store_b128(pack8(v), rws, (((c.w & 1) * 4 + c.g) * 32 + th * 16 + c.tok) * 16, dst, 0); }
  } else if (c.w < 6) {
    gemm_raw<true>(acc, wq, sm, L_ACT0, c);
#pragma unroll
    for (int th = 0; th < 2; th++) {
      auto r0 = __builtin_amdgcn_permlane32_swap(pack2(acc[0][th][0], acc[0][th][1]), pack2(acc[1][th][0], acc[1][th][1]), false, false);
      auto r1 = __builtin_amdgcn_permlane32_swap(pack2(acc[0][th][2], acc[0][th][3]), pack2(acc[1][th][2], acc[1][th][3]), false, false);
      u32x4 o = {r0[0], r1[0], r0[1], r1[1]};
      __builtin_amdgcn_raw_buffer_store_b128(o, rws, (c.tok + 16 * (c.lane >> 5) + 32 * (c.g & 1)) * 16 + ((c.w & 1) * 2 + th) * 1024, kvb + 4096, 0);
    }
  }
  STAMP(1, 1);
}

KATTR void k3(Params p) {
  __shared__ __attribute__((aligned(16))) char sm[L_END];
  STAMP(2, 0);
  const Ctx c = mk_ctx();
  rsrc_t rws = mk_rsrc(p.ws, WS_END);
  u32x4 qf[4], kf[4][4], vf[4][4];
#pragma unroll
  for (int ks = 0; ks < 4; ks++) qf[ks] = __builtin_amdgcn_raw_buffer_load_b128(rws, c.lane * 16 + ks * 1024, WS_Q + (c.tg * 4 + c.s) * 4096, 0);
  const unsigned kvb = WS_KV + ((c.b * 4 + c.s) * 32 + c.w * 4) * 8192;
#pragma unroll
  for (int t = 0; t < 4; t++)
#pragma unroll
    for (int ks = 0; ks < 4; ks++) kf[t][ks] = __builtin_amdgcn_raw_buffer_load_b128(rws, c.lane * 16 + ks * 1024, kvb + t * 8192, 0);
#pragma unroll
  for (int t = 0; t < 4; t++)
#pragma unroll
    for (int ks = 0; ks < 4; ks++) vf[t][ks] = __builtin_amdgcn_raw_buffer_load_b128(rws, c.lane * 16 + ks * 1024, kvb + t * 8192 + 4096, 0);
  SB();
  f32x16 S[4];
#pragma unroll
  for (int t = 0; t < 4; t++) {
#pragma unroll
    for (int e = 0; e < 16; e++) S[t][e] = 0.f;
#pragma unroll
    for (int ks = 0; ks < 4; ks++) S[t] = mfma32(kf[t][ks], qf[ks], S[t]);
  }
  if (c.w == 7) {
#pragma unroll
    for (int e = 4; e < 16; e++) S[3][e] = -INFINITY;
  }
  float m = S[0][0];
#pragma unroll
  for (int t = 0; t < 4; t++)
#pragma unroll
    for (int e = 0; e < 16; e++) m = fmaxf(m, S[t][e]);
  { auto r = __builtin_amdgcn_permlane32_swap(__float_as_uint(m), __float_as_uint(m), false, false); m = fmaxf(__uint_as_float(r[0]), __uint_as_float(r[1])); }
  const float CS = 0.125f * LOG2E;
  const float mc = -m * CS;
  float l = 0.f;
  f32x16 O[2];
#pragma unroll
  for (int e = 0; e < 16; e++) { O[0][e] = 0.f; O[1][e] = 0.f; }
#pragma unroll
  for (int t = 0; t < 4; t++) {
#pragma unroll
    for (int e = 0; e < 16; e++) { S[t][e] = __builtin_amdgcn_exp2f(fmaf(S[t][e], CS, mc)); l += S[t][e]; }
#pragma unroll
    for (int s2 = 0; s2 < 2; s2++) {
      u32x4 pf = {pack2(S[t][8 * s2], S[t][8 * s2 + 1]), pack2(S[t][8 * s2 + 2], S[t][8 * s2 + 3]), pack2(S[t][8 * s2 + 4], S[t][8 * s2 + 5]), pack2(S[t][8 * s2 + 6], S[t][8 * s2 + 7])};
      O[0] = mfma32(vf[t][s2], pf, O[0]);
      O[1] = mfma32(vf[t][2 + s2], pf, O[1]);
    }
  }
  l = swadd32(l, l);
  if (c.lane < 32) *(f32x2*)(sm + L_ML + (c.w * 32 + c.lane) * 8) = f32x2{m, l};
#pragma unroll
  for (int dt = 0; dt < 2; dt++)
#pragma unroll
    for (int q = 0; q < 4; q++) *(f32x4*)(sm + L_COMB + ((c.w * 8 + dt * 4 + q) * 64 + c.lane) * 16) = f32x4{O[dt][4 * q], O[dt][4 * q + 1], O[dt][4 * q + 2], O[dt][4 * q + 3]};
  __syncthreads();
  f32x2 ml[8]; float M = -INFINITY;
#pragma unroll
  for (int k = 0; k < 8; k++) { ml[k] = *(f32x2*)(sm + L_ML + (k * 32 + (c.lane & 31)) * 8); M = fmaxf(M, ml[k][0]); }
  float L = 0.f; f32x4 o = {0.f, 0.f, 0.f, 0.f};
#pragma unroll
  for (int k = 0; k < 8; k++) {
    float f = __builtin_amdgcn_exp2f((ml[k][0] - M) * CS);
    L = fmaf(f, ml[k][1], L);
    o += f * *(f32x4*)(sm + L_COMB + ((k * 8 + c.w) * 64 + c.lane) * 16);
  }
  o *= __builtin_amdgcn_rcpf(L);
  __builtin_amdgcn_raw_buffer_store_b64(u32x2{pack2(o[0], o[1]), pack2(o[2], o[3])}, rws, (c.w * 32 + (c.lane & 31)) * 16 + (c.lane >> 5) * 8, WS_ATT + (c.tg * 4 + c.s) * 4096, 0);
  STAMP(2, 1);
}

KATTR void k4(Params p) {
  __shared__ __attribute__((aligned(16))) char sm[L_Q];
  STAMP(3, 0);
  const Ctx c = mk_ctx();
  rsrc_t rws = mk_rsrc(p.ws, WS_END);
  u32x4 at[2], xr[2];
#pragma unroll
  for (int h = 0; h < 2; h++) at[h] = __builtin_amdgcn_raw_buffer_load_b128(rws, c.tid * 16 + h * 8192, WS_ATT + c.tg * 16384, 0);
#pragma unroll
  for (int th = 0; th < 2; th++) xr[th] = __builtin_amdgcn_raw_buffer_load_b128(rws, c.tid * 16 + th * 8192, WS_X + c.tg * 16384, 0);
  u32x4 wo[16], w1[8];
  load_tiled(wo, rws, c.lane * 16, WS_WT + WT_WO + c.w * 16384);
#pragma unroll
  for (int q = 0; q < 8; q++) w1[q] = __builtin_amdgcn_raw_buffer_load_b128(rws, c.lane * 16 + (q & 3) * 1024, WS_WT + WT_P1 + (c.s * 8 + c.w) * 8192 + (q >> 2) * 4096, 0);
  SB();
  lds_w128(sm, L_ACT1 + c.tid * 16, at[0]); lds_w128(sm, L_ACT1 + c.tid * 16 + 8192, at[1]);
  __syncthreads();
  f32x4 acc[2][2]; zero_acc(acc);
  gemm_tiled(acc, wo, sm, L_ACT1, c);
#pragma unroll
  for (int th = 0; th < 2; th++) {
    float x[8], a[8]; unpack8(x, xr[th]); acc_row(a, acc, th);
#pragma unroll
    for (int e = 0; e < 8; e++) x[e] += a[e];
    xr[th] = pack8(x);
    if (c.s == 0) __builtin_amdgcn_raw_buffer_store_b128(xr[th], rws, c.tid * 16 + th * 8192, WS_X + (1u << 20) + c.tg * 16384, 0);
  }
  ln_to_act(xr, sm, L_ACT0, L_STAT, c);
  f32x4 a2[2] = {f32x4{0.f, 0.f, 0.f, 0.f}, f32x4{0.f, 0.f, 0.f, 0.f}};
  const int base = L_ACT0 + (c.g * 32 + c.tok) * 16;
#pragma unroll
  for (int st = 0; st < 8; st++) {
    a2[0] = mfma16(lds_r128(sm, base + st * 2048), w1[st], a2[0]);
    a2[1] = mfma16(lds_r128(sm, base + st * 2048 + 256), w1[st], a2[1]);
  }
  const bool isA = c.tok < 8;
  const int th = c.tok >> 3;
  float gv[4];
#pragma unroll
  for (int j = 0; j < 4; j++) {
    float snd = isA ? a2[1][j] : a2[0][j];
    float rcv = __uint_as_float(__builtin_amdgcn_update_dpp(0, __float_as_uint(snd), 0x128, 0xf, 0xf, false));
    float a = isA ? a2[0][j] : rcv, b = isA ? rcv : a2[1][j];
    gv[j] = (c.tile * 32 + th * 16 + c.g * 4 + j < T_LEN) ? a * sigm_f(b) : 0.f;
  }
  __builtin_amdgcn_raw_buffer_store_b64(u32x2{pack2(gv[0], gv[1]), pack2(gv[2], gv[3])}, rws, th * 8192 + (c.s * 64 + c.w * 8 + (c.tok & 7)) * 32 + c.g * 8, WS_G + (c.b * 64 + c.tile * 2) * 8192, 0);
  STAMP(3, 1);
}

KATTR void k5(Params p) {
  __shared__ __attribute__((aligned(16))) char sm[L_COMB + 256 * G_ROW];
  STAMP(4, 0);
  const Ctx c = mk_ctx();
  rsrc_t rws = mk_rsrc(p.ws, WS_END);
  u32x4 gi[4], xr[2];
#pragma unroll
  for (int q = 0; q < 4; q++) {
    int ht = c.tile * 2 - 1 + q;
    gi[q] = __builtin_amdgcn_raw_buffer_load_b128(rws, (unsigned)ht < 64u ? WS_G + (c.b * 64 + ht) * 8192 + c.tid * 16 : 0x40000000, 0, 0);
  }
  u32x4 dwf[2][5];
#pragma unroll
  for (int gq = 0; gq < 2; gq++)
#pragma unroll
    for (int m = 0; m < 5; m++) dwf[gq][m] = __builtin_amdgcn_raw_buffer_load_b128(rws, c.lane * 16, WS_WT + WT_DW + ((c.w * 2 + gq) * 5 + m) * 1024, 0);
#pragma unroll
  for (int th = 0; th < 2; th++) xr[th] = __builtin_amdgcn_raw_buffer_load_b128(rws, c.tid * 16 + th * 8192, WS_X + (1u << 20) + c.tg * 16384, 0);
  u32x4 wp[16], wa[16];
  load_tiled(wp, rws, c.lane * 16, WS_WT + WT_P2 + c.w * 16384);
  SB();
#pragma unroll
  for (int q = 0; q < 4; q++) lds_w128(sm, L_COMB + (c.tid >> 1) * G_ROW + q * 32 + (c.tid & 1) * 16, gi[q]);
  __syncthreads();
  load_tiled(wa, rws, c.lane * 16, WS_WT + WT_2A + (c.s * 8 + c.w) * 16384);
  SB();
#pragma unroll
  for (int gq = 0; gq < 2; gq++) {
    const int slot = (c.w * 2 + gq) * 16 + (c.lane >> 2), n = c.lane & 3;
    u32x4 in[5];
#pragma unroll
    for (int m = 0; m < 5; m++) in[m] = lds_r128(sm, L_COMB + slot * G_ROW + n * 16 + m * 16);
    f32x4 d0 = {0.f, 0.f, 0.f, 0.f}, d1 = {0.f, 0.f, 0.f, 0.f};
#pragma unroll
    for (int j = 0; j < 9; j++) {
      u32x2 a = {dwf[gq][j >> 1][(j & 1) * 2], dwf[gq][j >> 1][(j & 1) * 2 + 1]};
      u32x2 b0 = {in[j >> 1][(j & 1) * 2], in[j >> 1][(j & 1) * 2 + 1]};
      u32x2 b1 = {in[(j + 1) >> 1][((j + 1) & 1) * 2], in[(j + 1) >> 1][((j + 1) & 1) * 2 + 1]};
      d0 = mfma4(a, b0, d0); d1 = mfma4(a, b1, d1);
    }
    u32x4 o = {pack2(silu_f(d0[0]), silu_f(d0[1])), pack2(silu_f(d0[2]), silu_f(d0[3])), pack2(silu_f(d1[0]), silu_f(d1[1])), pack2(silu_f(d1[2]), silu_f(d1[3]))};
    lds_w128(sm, L_ACT1 + slot * 64 + (n ^ (((slot >> 3) & 1) * 2)) * 16, o);
  }
  __syncthreads();
  f32x4 acc[2][2]; zero_acc(acc);
  {
    const int q = c.tok >> 2, pp = c.tok & 3;
    const int base = L_ACT1 + (c.g * 8 + q) * 64 + pp * 8;
#pragma unroll
    for (int st = 0; st < 8; st++) {
#pragma unroll
      for (int th = 0; th < 2; th++) {
        const int o = base + st * 2048 + ((th ^ (c.g & 1)) * 32);
        u32x2 lo = __builtin_bit_cast(u32x2, __builtin_amdgcn_ds_read_tr16_b64_v4bf16((LAS bf16x4*)(sm + o)));
        u32x2 hi = __builtin_bit_cast(u32x2, __builtin_amdgcn_ds_read_tr16_b64_v4bf16((LAS bf16x4*)(sm + o + 256)));
        u32x4 bf = {lo[0], lo[1], hi[0], hi[1]};
        acc[0][th] = mfma16(wp[st * 2], bf, acc[0][th]);
        acc[1][th] = mfma16(wp[st * 2 + 1], bf, acc[1][th]);
      }
    }
  }
  SB();
  u32x4 wb[16];
  load_tiled(wb, rws, c.lane * 16, WS_WT + WT_2B + (c.s * 8 + c.w) * 16384);
  SB();
#pragma unroll
  for (int th = 0; th < 2; th++) {
    float x[8], a[8]; unpack8(x, xr[th]); acc_row(a, acc, th);
#pragma unroll
    for (int e = 0; e < 8; e++) x[e] += a[e];
    xr[th] = pack8(x);
    if (c.s == 0) __builtin_amdgcn_raw_buffer_store_b128(xr[th], rws, c.tid * 16 + th * 8192, WS_X + (2u << 20) + c.tg * 16384, 0);
  }
  ln_to_act(xr, sm, L_ACT0, L_STAT, c);
  zero_acc(acc);
  gemm_tiled(acc, wa, sm, L_ACT0, c);
  float v[2][8];
#pragma unroll
  for (int th = 0; th < 2; th++) { acc_row(v[th], acc, th);
#pragma unroll
    for (int e = 0; e < 8; e++) v[th][e] = silu_f(v[th][e]); }
  act_store(sm, L_ACT1, c, pack8(v[0]), pack8(v[1]));
  __syncthreads();
  zero_acc(acc);
  gemm_tiled(acc, wb, sm, L_ACT1, c);
#pragma unroll
  for (int th = 0; th < 2; th++) { acc_row(v[th], acc, th);
    const int dest = th * 2 + (c.tok >> 3);
    __builtin_amdgcn_raw_buffer_store_b128(pack8(v[th]), rws, dest * 16384 + (c.tok & 7) * 512 + (c.w * 32 + c.g * 8) * 2, WS_P2 + (c.tg * 16 + c.s) * 4096, 0); }
  STAMP(4, 1);
}

DI float wave_sum(float v) {
  v += __uint_as_float(__builtin_amdgcn_update_dpp(0, __float_as_uint(v), 0xB1, 0xf, 0xf, false));
  v += __uint_as_float(__builtin_amdgcn_update_dpp(0, __float_as_uint(v), 0x4E, 0xf, 0xf, false));
  v += __uint_as_float(__builtin_amdgcn_update_dpp(0, __float_as_uint(v), 0x124, 0xf, 0xf, false));
  v += __uint_as_float(__builtin_amdgcn_update_dpp(0, __float_as_uint(v), 0x128, 0xf, 0xf, false));
  v = swadd16(v, v); v = swadd32(v, v);
  return v;
}
KATTR void k6(Params p) {
  STAMP(5, 0);
  const Ctx c = mk_ctx();
  rsrc_t rws = mk_rsrc(p.ws, WS_END);
  const int tl = c.s * 8 + c.w;
  u32x2 pp[4];
#pragma unroll
  for (int s = 0; s < 4; s++) pp[s] = __builtin_amdgcn_raw_buffer_load_b64(rws, c.w * 512 + c.lane * 8, WS_P2 + (c.tg * 16 + c.s * 4 + s) * 4096, 0);
  const int f = c.lane * 4;
  u32x2 xx = __builtin_amdgcn_raw_buffer_load_b64(rws, (tl >> 4) * 8192 + ((f >> 5) * 64 + ((f >> 3) & 3) * 16 + (tl & 15)) * 16 + (f & 4) * 2, WS_X + (2u << 20) + c.tg * 16384, 0);
  float a[4] = {0.f, 0.f, 0.f, 0.f};
#pragma unroll
  for (int s = 0; s < 4; s++) { a[0] += lo16(pp[s][0]); a[1] += hi16(pp[s][0]); a[2] += lo16(pp[s][1]); a[3] += hi16(pp[s][1]); }
  float x[4] = {fmaf(a[0], 0.5f, lo16(xx[0])), fmaf(a[1], 0.5f, hi16(xx[0])), fmaf(a[2], 0.5f, lo16(xx[1])), fmaf(a[3], 0.5f, hi16(xx[1]))};
  float mean = wave_sum(x[0] + x[1] + x[2] + x[3]) * (1.f / 256.f);
#pragma unroll
  for (int e = 0; e < 4; e++) x[e] -= mean;
  float var = wave_sum(x[0] * x[0] + x[1] * x[1] + x[2] * x[2] + x[3] * x[3]) * (1.f / 256.f);
  float rstd = __builtin_amdgcn_rsqf(var + 1e-5f);
  const int t = c.tile * 32 + tl;
  if (t < T_LEN) *(u32x2*)((char*)p.out + (size_t)(c.b * T_LEN + t) * 512 + c.lane * 8) = u32x2{pack2(x[0] * rstd, x[1] * rstd), pack2(x[2] * rstd, x[3] * rstd)};
  STAMP(5, 1);
}

extern "C" void kernel_launch(void* const* d_in, const int* in_sizes, int n_in, void* d_out, int out_size, void* d_ws, size_t ws_size, hipStream_t stream) {
  Params p;
  p.x = (const u16*)d_in[0]; p.w1a = (const u16*)d_in[1]; p.w1b = (const u16*)d_in[2]; p.wqkv = (const u16*)d_in[3]; p.wo = (const u16*)d_in[4];
  p.wp1 = (const u16*)d_in[5]; p.dw = (const u16*)d_in[6]; p.wp2 = (const u16*)d_in[7]; p.w2a = (const u16*)d_in[8]; p.w2b = (const u16*)d_in[9];
  p.out = (u16*)d_out; p.ws = (char*)d_ws;
  k1<<<256, 512, 0, stream>>>(p);
  k2<<<256, 512, 0, stream>>>(p);
  k3<<<256, 512, 0, stream>>>(p);
  k4<<<256, 512, 0, stream>>>(p);
  k5<<<256, 512, 0, stream>>>(p);
  k6<<<256, 512, 0, stream>>>(p);
}
```

```cpp
#include <hip/hip_runtime.h>
#include <hip/hip_bf16.h>

typedef unsigned short u16;
typedef unsigned long long u64;
typedef __attribute__((ext_vector_type(2))) unsigned u32x2;
typedef __attribute__((ext_vector_type(4))) unsigned u32x4;
typedef __attribute__((ext_vector_type(2))) float f32x2;
typedef __attribute__((ext_vector_type(4))) float f32x4;
typedef __attribute__((ext_vector_type(16))) float f32x16;
typedef __attribute__((ext_vector_type(2))) __bf16 bf16x2;
typedef __attribute__((ext_vector_type(4))) __bf16 bf16x4;
typedef __attribute__((ext_vector_type(8))) __bf16 bf16x8;
typedef __attribute__((ext_vector_type(4))) short s16x4;
typedef __amdgpu_buffer_rsrc_t rsrc_t;
#define DI __device__ __forceinline__
#define LAS __attribute__((address_space(3)))
#define SB() __builtin_amdgcn_sched_barrier(0)

constexpr int T_LEN = 1000;
constexpr float LOG2E = 1.4426950408889634f;

constexpr unsigned WS_P1  = 0;
constexpr unsigned WS_KV  = WS_P1 + (4u << 20);
constexpr unsigned WS_Q   = WS_KV + (2u << 20);
constexpr unsigned WS_ATT = WS_Q + (1u << 20);
constexpr unsigned WS_G   = WS_ATT + (1u << 20);
constexpr unsigned WS_X   = WS_G + (1u << 20);
constexpr unsigned WS_P2  = WS_X + (3u << 20);
constexpr unsigned WS_WT  = WS_P2 + (4u << 20);
constexpr unsigned WT_WO = 0, WT_P2 = 128u << 10, WT_2A = 256u << 10, WT_2B = 768u << 10, WT_P1 = 1280u << 10, WT_DW = 1536u << 10;
constexpr unsigned WS_SYNC = WS_WT + (4u << 20);
constexpr unsigned SY_E = 0, SY_F1 = 1024, SY_F2 = SY_F1 + 32768, SY_FP = SY_F2 + 32768;
constexpr unsigned WS_END = WS_SYNC + (1u << 20);
constexpr unsigned WS_PROF = 64u << 20;

constexpr int L_ACT0 = 0, L_ACT1 = 16384, L_STAT = 32768, L_Q = 36864, L_COMB = 40960, L_ML = L_COMB + 65536, L_END = L_ML + 2048;
constexpr int G_ROW = 144;

DI rsrc_t mk_rsrc(const void* p, unsigned bytes) { return __builtin_amdgcn_make_buffer_rsrc((void*)p, 0, bytes, 0x00020000); }
DI unsigned pack2(float a, float b) { bf16x2 v = {(__bf16)a, (__bf16)b}; return __builtin_bit_cast(unsigned, v); }
DI float lo16(unsigned u) { return __uint_as_float(u << 16); }
DI float hi16(unsigned u) { return __uint_as_float(u & 0xffff0000u); }
DI f32x4 mfma16(u32x4 a, u32x4 b, f32x4 c) { return __builtin_amdgcn_mfma_f32_16x16x32_bf16(__builtin_bit_cast(bf16x8, a), __builtin_bit_cast(bf16x8, b), c, 0, 0, 0); }
DI f32x16 mfma32(u32x4 a, u32x4 b, f32x16 c) { return __builtin_amdgcn_mfma_f32_32x32x16_bf16(__builtin_bit_cast(bf16x8, a), __builtin_bit_cast(bf16x8, b), c, 0, 0, 0); }
DI f32x4 mfma4(u32x2 a, u32x2 b, f32x4 c) { return __builtin_amdgcn_mfma_f32_4x4x4bf16_1k(__builtin_bit_cast(s16x4, a), __builtin_bit_cast(s16x4, b), c, 0, 0, 0); }
DI float silu_f(float x) { return x * __builtin_amdgcn_rcpf(1.f + __builtin_amdgcn_exp2f(-LOG2E * x)); }
DI float sigm_f(float x) { return __builtin_amdgcn_rcpf(1.f + __builtin_amdgcn_exp2f(-LOG2E * x)); }
DI u32x4 lds_r128(char* sm, int off) { return *(u32x4*)(sm + off); }
DI void lds_w128(char* sm, int off, u32x4 v) { *(u32x4*)(sm + off) = v; }
DI float swadd32(float a, float b) { auto r = __builtin_amdgcn_permlane32_swap(__float_as_uint(a), __float_as_uint(b), false, false); return __uint_as_float(r[0]) + __uint_as_float(r[1]); }
DI float swadd16(float a, float b) { auto r = __builtin_amdgcn_permlane16_swap(__float_as_uint(a), __float_as_uint(b), false, false); return __uint_as_float(r[0]) + __uint_as_float(r[1]); }

struct Ctx { int tid, w, lane, tok, g, b, tile, s, tg; };
DI Ctx mk_ctx() {
  Ctx c; c.tid = threadIdx.x; c.w = __builtin_amdgcn_readfirstlane(threadIdx.x >> 6); c.lane = c.tid & 63; c.tok = c.lane & 15; c.g = c.lane >> 4;
  int bid = blockIdx.x; c.s = (bid >> 3) & 3; c.tg = (bid & 7) * 8 + (bid >> 5); c.b = c.tg >> 5; c.tile = c.tg & 31; return c;
}

template <int LD> DI void load_raw(unsigned (&raw)[64], rsrc_t rs, int voff) {
#pragma unroll
  for (int st = 0; st < 8; st++)
#pragma unroll
    for (int j = 0; j < 8; j++) raw[st * 8 + j] = __builtin_amdgcn_raw_buffer_load_b32(rs, voff, (st * 32 + j) * LD, 0);
}
DI u32x4 tr_frag(const unsigned* r8, int i) {
  const unsigned sel = i ? 0x07060302u : 0x05040100u;
  u32x4 f;
#pragma unroll
  for (int m = 0; m < 4; m++) f[m] = __builtin_amdgcn_perm(r8[2 * m + 1], r8[2 * m], sel);
  return f;
}
DI void load_tiled(u32x4 (&fr)[16], rsrc_t rs, int voff, int soff) {
#pragma unroll
  for (int q = 0; q < 16; q++) fr[q] = __builtin_amdgcn_raw_buffer_load_b128(rs, voff + (q & 3) * 1024, soff + (q >> 2) * 4096, 16);
}
template <bool SWAP> DI void gemm_raw(f32x4 (&acc)[2][2], const unsigned (&raw)[64], char* sm, int act, const Ctx& c) {
  const int base = act + (c.g * 32 + c.tok) * 16;
#pragma unroll
  for (int st = 0; st < 8; st++) {
    u32x4 b0 = lds_r128(sm, base + st * 2048), b1 = lds_r128(sm, base + st * 2048 + 256);
#pragma unroll
    for (int i = 0; i < 2; i++) {
      u32x4 a = tr_frag(&raw[st * 8], i);
      acc[i][0] = SWAP ? mfma16(b0, a, acc[i][0]) : mfma16(a, b0, acc[i][0]);
      acc[i][1] = SWAP ? mfma16(b1, a, acc[i][1]) : mfma16(a, b1, acc[i][1]);
    }
  }
}
DI void gemm_tiled(f32x4 (&acc)[2][2], const u32x4 (&fr)[16], char* sm, int act, const Ctx& c) {
  const int base = act + (c.g * 32 + c.tok) * 16;
#pragma unroll
  for (int st = 0; st < 8; st++) {
    u32x4 b0 = lds_r128(sm, base + st * 2048), b1 = lds_r128(sm, base + st * 2048 + 256);
#pragma unroll
    for (int i = 0; i < 2; i++) {
      acc[i][0] = mfma16(fr[st * 2 + i], b0, acc[i][0]);
      acc[i][1] = mfma16(fr[st * 2 + i], b1, acc[i][1]);
    }
  }
}
DI void zero_acc(f32x4 (&acc)[2][2]) {
#pragma unroll
  for (int i = 0; i < 2; i++)
#pragma unroll
    for (int t = 0; t < 2; t++) acc[i][t] = f32x4{0.f, 0.f, 0.f, 0.f};
}
DI void unpack8(float (&v)[8], u32x4 p) {
#pragma unroll
  for (int m = 0; m < 4; m++) { v[2 * m] = lo16(p[m]); v[2 * m + 1] = hi16(p[m]); }
}
DI u32x4 pack8(const float (&v)[8]) { return u32x4{pack2(v[0], v[1]), pack2(v[2], v[3]), pack2(v[4], v[5]), pack2(v[6], v[7])}; }
DI void act_store(char* sm, int act, const Ctx& c, u32x4 p0, u32x4 p1) {
  const int o = act + ((c.w * 4 + c.g) * 32 + c.tok) * 16;
  lds_w128(sm, o, p0); lds_w128(sm, o + 256, p1);
}
DI void ln_to_act(const u32x4 (&xr)[2], char* sm, int act, int stat, const Ctx& c) {
  float v[2][8]; unpack8(v[0], xr[0]); unpack8(v[1], xr[1]);
  float s0 = 0, s1 = 0, q0 = 0, q1 = 0;
#pragma unroll
  for (int e = 0; e < 8; e++) { s0 += v[0][e]; s1 += v[1][e]; q0 = fmaf(v[0][e], v[0][e], q0); q1 = fmaf(v[1][e], v[1][e], q1); }
  float t = swadd16(swadd32(s0, s1), swadd32(q0, q1));
  *(float*)(sm + stat + (c.w * 64 + c.lane) * 4) = t;
  __syncthreads();
  float u = 0;
#pragma unroll
  for (int k = 0; k < 8; k++) u += *(float*)(sm + stat + (k * 64 + c.lane) * 4);
  auto r = __builtin_amdgcn_permlane16_swap(__float_as_uint(u), __float_as_uint(u), false, false);
  float mean = __uint_as_float(r[0]) * (1.f / 256.f);
  float var = fmaxf(fmaf(-mean, mean, __uint_as_float(r[1]) * (1.f / 256.f)), 0.f);
  float rstd = __builtin_amdgcn_rsqf(var + 1e-5f), nm = -mean * rstd;
  auto rr = __builtin_amdgcn_permlane32_swap(__float_as_uint(rstd), __float_as_uint(rstd), false, false);
  auto rn = __builtin_amdgcn_permlane32_swap(__float_as_uint(nm), __float_as_uint(nm), false, false);
#pragma unroll
  for (int t2 = 0; t2 < 2; t2++)
#pragma unroll
    for (int e = 0; e < 8; e++) v[t2][e] = fmaf(v[t2][e], __uint_as_float(rr[t2]), __uint_as_float(rn[t2]));
  act_store(sm, act, c, pack8(v[0]), pack8(v[1]));
  __syncthreads();
}
DI void acc_row(float (&v)[8], const f32x4 (&acc)[2][2], int th) {
#pragma unroll
  for (int j = 0; j < 4; j++) { v[2 * j] = acc[0][th][j]; v[2 * j + 1] = acc[1][th][j]; }
}
DI int chmap(int slot) { return (slot & 0xC0) + ((slot >> 4) & 3) * 16 + (slot & 7) * 2 + ((slot >> 3) & 1); }

struct Params { const u16 *x, *w1a, *w1b, *wqkv, *wo, *wp1, *dw, *wp2, *w2a, *w2b; u16* out; char* ws; };

template <int AUX> DI void prep_task(const Params& p, int task, rsrc_t rws, unsigned wt, int lane) {
  const int r = lane & 15, g = lane >> 4;
  if (task < 768) {
    const u16* src; int ld, rowbase, jst, col; unsigned d0, d1; unsigned bytes;
    const int st = task & 7;
    if (task < 64) { const int u = task >> 3, C = 4 * st + g, quad = C & 3; src = p.wo; ld = 512; bytes = 131072; rowbase = 32 * (C >> 2) + 16 * (quad & 1) + (quad >> 1); jst = 2; col = 32 * u + 2 * r; d0 = WT_WO + (task * 2) * 1024; d1 = d0 + 1024; }
    else if (task < 128) { const int t = task - 64, u = t >> 3, w = 4 * (st & 1) + g; src = p.wp2; ld = 512; bytes = 131072; rowbase = 64 * (st >> 1) + 16 * (w >> 1) + (w & 1); jst = 2; col = 32 * u + 2 * r; d0 = WT_P2 + (t * 2) * 1024; d1 = d0 + 1024; }
    else if (task < 384) { const int t = task - 128, su = t >> 3; src = p.w2a; ld = 2048; bytes = 524288; rowbase = 32 * st + 8 * g; jst = 1; col = 32 * su + 2 * r; d0 = WT_2A + (t * 2) * 1024; d1 = d0 + 1024; }
    else if (task < 640) { const int t = task - 384, su = t >> 3; src = p.w2b; ld = 512; bytes = 524288; rowbase = 256 * (su >> 3) + 32 * st + 8 * g; jst = 1; col = 32 * (su & 7) + 2 * r; d0 = WT_2B + (t * 2) * 1024; d1 = d0 + 1024; }
    else { const int t = task - 640, v = t >> 3; src = p.wp1; ld = 1024; bytes = 262144; rowbase = 32 * st + 8 * g; jst = 1; col = 16 * v + 2 * (r & 7) + (r >> 3) * 256; d0 = WT_P1 + ((2 * v) * 8 + st) * 1024; d1 = d0 + 8192; }
    rsrc_t rs = mk_rsrc(src, bytes);
    unsigned raw[8];
#pragma unroll
    for (int j = 0; j < 8; j++) raw[j] = __builtin_amdgcn_raw_buffer_load_b32(rs, rowbase * ld + col * 2, j * jst * ld, 0);
    __builtin_amdgcn_raw_buffer_store_b128(tr_frag(raw, 0), rws, lane * 16, wt + d0, AUX);
    __builtin_amdgcn_raw_buffer_store_b128(tr_frag(raw, 1), rws, lane * 16, wt + d1, AUX);
  } else if (task < 848) {
    const int t = task - 768, grp = t / 5, m = t - grp * 5;
    const int ch = chmap(16 * grp + (lane >> 2)), i = lane & 3;
    rsrc_t rs = mk_rsrc(p.dw, 31 * 512);
    unsigned h[8];
#pragma unroll
    for (int e = 0; e < 8; e++) { int tap = 8 * m + e - 1 - i; h[e] = __builtin_amdgcn_raw_buffer_load_b16(rs, (unsigned)tap < 31u ? tap * 512 + ch * 2 : 0x100000, 0, 0); }
    u32x4 o = {h[0] | (h[1] << 16), h[2] | (h[3] << 16), h[4] | (h[5] << 16), h[6] | (h[7] << 16)};
    __builtin_amdgcn_raw_buffer_store_b128(o, rws, lane * 16, wt + WT_DW + t * 1024, AUX);
  }
}

DI void load_x(u32x4 (&xr)[2], const Params& p, const Ctx& c) {
  rsrc_t rx = mk_rsrc(p.x, 2000 * 512);
#pragma unroll
  for (int th = 0; th < 2; th++) {
    int t = c.tile * 32 + th * 16 + c.tok;
    int off = t < T_LEN ? (c.b * T_LEN + t) * 512 + (c.w * 32 + c.g * 8) * 2 : 0x10000000;
    xr[th] = __builtin_amdgcn_raw_buffer_load_b128(rx, off, 0, 0);
  }
}
#define TS_DECL
#define TS(i)
#define TS_OUT(k)
#define VMW()
#define KATTR __global__ __launch_bounds__(512) __attribute__((amdgpu_waves_per_eu(2, 2)))

DI float wave_sum(float v) {
  v += __uint_as_float(__builtin_amdgcn_update_dpp(0, __float_as_uint(v), 0xB1, 0xf, 0xf, false));
  v += __uint_as_float(__builtin_amdgcn_update_dpp(0, __float_as_uint(v), 0x4E, 0xf, 0xf, false));
  v += __uint_as_float(__builtin_amdgcn_update_dpp(0, __float_as_uint(v), 0x124, 0xf, 0xf, false));
  v += __uint_as_float(__builtin_amdgcn_update_dpp(0, __float_as_uint(v), 0x128, 0xf, 0xf, false));
  v = swadd16(v, v); v = swadd32(v, v);
  return v;
}

constexpr int SPIN_CAP = 1 << 17;
#define DRAIN() asm volatile("s_waitcnt vmcnt(0)" ::: "memory")
DI u32x2 ld_flag(rsrc_t rws, int off) { return __builtin_amdgcn_raw_buffer_load_b64(rws, off, WS_SYNC, 16); }
DI void st_flag(rsrc_t rws, int off, u32x2 v) { __builtin_amdgcn_raw_buffer_store_b64(v, rws, off, WS_SYNC, 16); }
DI void poll_flags(rsrc_t rws, int off, u32x2 ep, bool& dead) {
  if (dead) return;
  for (int sp = 0;; sp++) {
    u32x2 f = ld_flag(rws, off);
    if (__all(f[0] == ep[0] && f[1] == ep[1])) break;
    if (sp > SPIN_CAP) { dead = true; break; }
    asm volatile("" ::: "memory");
  }
  asm volatile("" ::: "memory");
}
DI unsigned umax4(u32x4 v) { return max(max(v[0], v[1]), max(v[2], v[3])); }
DI unsigned san(unsigned v) { return min(v, 0xFFFFFFFEu); }

KATTR void k_all(Params p) {
  __shared__ __attribute__((aligned(16))) char sm[L_END];
  TS_DECL; TS(0);
  const Ctx c = mk_ctx();
  rsrc_t rws = mk_rsrc(p.ws, WS_END);
  const unsigned wt = WS_WT + c.b * (2u << 20);
  bool dead = false;
  u32x2 ep = ld_flag(rws, SY_E + c.b * 128);
  u32x4 xr[2]; load_x(xr, p, c);
  f32x4 acc[2][2];
  float v[2][8];
  {
    unsigned wa[64], wb[64];
    load_raw<2048>(wa, mk_rsrc(p.w1a, 524288), c.g * 8 * 2048 + (c.s * 256 + c.w * 32 + c.tok * 2) * 2);
    SB(); TS(1);
    ln_to_act(xr, sm, L_ACT0, L_STAT, c);
    SB(); TS(2);
    load_raw<512>(wb, mk_rsrc(p.w1b, 524288), (c.s * 256 + c.g * 8) * 512 + (c.w * 32 + c.tok * 2) * 2);
    SB();
    zero_acc(acc);
    gemm_raw<false>(acc, wa, sm, L_ACT0, c);
#pragma unroll
    for (int th = 0; th < 2; th++) { acc_row(v[th], acc, th);
#pragma unroll
      for (int e = 0; e < 8; e++) v[th][e] = silu_f(v[th][e]); }
    act_store(sm, L_ACT1, c, pack8(v[0]), pack8(v[1]));
    __syncthreads();
    TS(3);
    zero_acc(acc);
    gemm_raw<false>(acc, wb, sm, L_ACT1, c);
  }
  { u64 e = (((u64)ep[1] << 32) | ep[0]) + 1; ep[0] = __builtin_amdgcn_readfirstlane((unsigned)e); ep[1] = __builtin_amdgcn_readfirstlane((unsigned)(e >> 32)); }
  u32x4 own[2];
#pragma unroll
  for (int th = 0; th < 2; th++) { acc_row(v[th], acc, th); own[th] = pack8(v[th]);
    __builtin_amdgcn_raw_buffer_store_b128(own[th], rws, c.tid * 16 + th * 8192, WS_P1 + (c.tg * 4 + c.s) * 16384, 16); }
  TS(4);
  DRAIN();
  __syncthreads();
  if (c.tid == 0) st_flag(rws, SY_F1 + (c.tg * 4 + c.s) * 128, ep);
  TS(5);
  {
    const u32x4 ff = {~0u, ~0u, ~0u, ~0u};
    if (c.tid < 256) __builtin_amdgcn_raw_buffer_store_b128(ff, rws, c.tid * 16, WS_ATT + (c.tg * 4 + c.s) * 4096, 16);
    else __builtin_amdgcn_raw_buffer_store_b128(ff, rws, ((c.tid >> 7) & 1) * 8192 + c.s * 2048 + (c.tid & 127) * 16, WS_G + (c.b * 64 + c.tile * 2) * 8192, 16);
#pragma unroll
    for (int h = 0; h < 2; h++) __builtin_amdgcn_raw_buffer_store_b128(ff, rws, (h * 2 + (c.tid >> 8)) * 16384 + (c.tid & 255) * 16, WS_P2 + (c.tg * 16 + c.s) * 4096, 16);
    prep_task<16>(p, c.w * 128 + c.tile * 4 + c.s, rws, wt, c.lane);
  }
  DRAIN();
  __syncthreads();
  if (c.tid == 0) st_flag(rws, SY_FP + (c.tg * 4 + c.s) * 128, ep);
  TS(6);
  poll_flags(rws, SY_F1 + (c.tg * 4 + (c.lane & 3)) * 128, ep, dead);
  TS(7);
  {
    u32x4 pp[4][2];
#pragma unroll
    for (int s = 0; s < 4; s++)
#pragma unroll
      for (int th = 0; th < 2; th++) pp[s][th] = __builtin_amdgcn_raw_buffer_load_b128(rws, s == c.s ? 0x40000000 : c.tid * 16 + th * 8192, WS_P1 + (c.tg * 4 + s) * 16384, 16);
    unsigned wq[64];
    if (c.w < 6) load_raw<1536>(wq, mk_rsrc(p.wqkv, 393216), c.g * 8 * 1536 + ((c.w >> 1) * 256 + c.s * 64 + (c.w & 1) * 32 + c.tok * 2) * 2);
    SB(); TS(8);
#pragma unroll
    for (int th = 0; th < 2; th++) {
      float x[8], a[8], t[8]; unpack8(x, xr[th]); unpack8(a, own[th]);
#pragma unroll
      for (int s = 0; s < 4; s++) { unpack8(t, pp[s][th]);
#pragma unroll
        for (int e = 0; e < 8; e++) a[e] += t[e]; }
#pragma unroll
      for (int e = 0; e < 8; e++) x[e] = fmaf(a[e], 0.5f, x[e]);
      xr[th] = pack8(x);
    }
    TS(9);
    ln_to_act(xr, sm, L_ACT0, L_STAT, c);
    TS(10);
    zero_acc(acc);
    const unsigned kvb = WS_KV + ((c.b * 4 + c.s) * 32 + c.tile) * 8192;
    if (c.w < 4) {
      gemm_raw<false>(acc, wq, sm, L_ACT0, c);
#pragma unroll
      for (int th = 0; th < 2; th++) { acc_row(v[th], acc, th);
        const int o = (((c.w & 1) * 4 + c.g) * 32 + th * 16 + c.tok) * 16;
        if (c.w < 2) lds_w128(sm, L_Q + o, pack8(v[th]));
        else __builtin_amdgcn_raw_buffer_store_b128(pack8(v[th]), rws, o, kvb, 16); }
    } else if (c.w < 6) {
      gemm_raw<true>(acc, wq, sm, L_ACT0, c);
#pragma unroll
      for (int th = 0; th < 2; th++) {
        auto r0 = __builtin_amdgcn_permlane32_swap(pack2(acc[0][th][0], acc[0][th][1]), pack2(acc[1][th][0], acc[1][th][1]), false, false);
        auto r1 = __builtin_amdgcn_permlane32_swap(pack2(acc[0][th][2], acc[0][th][3]), pack2(acc[1][th][2], acc[1][th][3]), false, false);
        u32x4 o = {r0[0], r1[0], r0[1], r1[1]};
        __builtin_amdgcn_raw_buffer_store_b128(o, rws, (c.tok + 16 * (c.lane >> 5) + 32 * (c.g & 1)) * 16 + ((c.w & 1) * 2 + th) * 1024, kvb + 4096, 16);
      }
    }
  }
  TS(11);
  DRAIN();
  __syncthreads();
  if (c.tid == 0) st_flag(rws, SY_F2 + ((c.b * 4 + c.s) * 32 + c.tile) * 128, ep);
  TS(12);
  poll_flags(rws, SY_FP + (c.b * 128 + c.lane) * 128, ep, dead);
  poll_flags(rws, SY_FP + (c.b * 128 + 64 + c.lane) * 128, ep, dead);
  if (c.tid == 0 && c.tile == 0 && c.s == 0) st_flag(rws, SY_E + c.b * 128, ep);
  TS(13);
  {
    u32x4 qf[4], kf[4][4], vf[4][4];
#pragma unroll
    for (int ks = 0; ks < 4; ks++) qf[ks] = lds_r128(sm, L_Q + c.lane * 16 + ks * 1024);
    poll_flags(rws, SY_F2 + ((c.b * 4 + c.s) * 32 + c.w * 4 + (c.lane & 3)) * 128, ep, dead);
    TS(14);
    const unsigned kvb = WS_KV + ((c.b * 4 + c.s) * 32 + c.w * 4) * 8192;
#pragma unroll
    for (int t = 0; t < 4; t++)
#pragma unroll
      for (int ks = 0; ks < 4; ks++) kf[t][ks] = __builtin_amdgcn_raw_buffer_load_b128(rws, c.lane * 16 + ks * 1024, kvb + t * 8192, 16);
#pragma unroll
    for (int t = 0; t < 4; t++)
#pragma unroll
      for (int ks = 0; ks < 4; ks++) vf[t][ks] = __builtin_amdgcn_raw_buffer_load_b128(rws, c.lane * 16 + ks * 1024, kvb + t * 8192 + 4096, 16);
    SB(); TS(15);
    f32x16 S[4];
#pragma unroll
    for (int t = 0; t < 4; t++) {
#pragma unroll
      for (int e = 0; e < 16; e++) S[t][e] = 0.f;
#pragma unroll
      for (int ks = 0; ks < 4; ks++) S[t] = mfma32(kf[t][ks], qf[ks], S[t]);
    }
    if (c.w == 7) {
#pragma unroll
      for (int e = 4; e < 16; e++) S[3][e] = -INFINITY;
    }
    float m = S[0][0];
#pragma unroll
    for (int t = 0; t < 4; t++)
#pragma unroll
      for (int e = 0; e < 16; e++) m = fmaxf(m, S[t][e]);
    { auto r = __builtin_amdgcn_permlane32_swap(__float_as_uint(m), __float_as_uint(m), false, false); m = fmaxf(__uint_as_float(r[0]), __uint_as_float(r[1])); }
    const float CS = 0.125f * LOG2E;
    const float mc = -m * CS;
    float l = 0.f;
    f32x16 O[2];
#pragma unroll
    for (int e = 0; e < 16; e++) { O[0][e] = 0.f; O[1][e] = 0.f; }
#pragma unroll
    for (int t = 0; t < 4; t++) {
#pragma unroll
      for (int e = 0; e < 16; e++) { S[t][e] = __builtin_amdgcn_exp2f(fmaf(S[t][e], CS, mc)); l += S[t][e]; }
#pragma unroll
      for (int s2 = 0; s2 < 2; s2++) {
        u32x4 pf = {pack2(S[t][8 * s2], S[t][8 * s2 + 1]), pack2(S[t][8 * s2 + 2], S[t][8 * s2 + 3]), pack2(S[t][8 * s2 + 4], S[t][8 * s2 + 5]), pack2(S[t][8 * s2 + 6], S[t][8 * s2 + 7])};
        O[0] = mfma32(vf[t][s2], pf, O[0]);
        O[1] = mfma32(vf[t][2 + s2], pf, O[1]);
      }
    }
    l = swadd32(l, l);
    if (c.lane < 32) *(f32x2*)(sm + L_ML + (c.w * 32 + c.lane) * 8) = f32x2{m, l};
#pragma unroll
    for (int dt = 0; dt < 2; dt++)
#pragma unroll
      for (int q = 0; q < 4; q++) *(f32x4*)(sm + L_COMB + ((c.w * 8 + dt * 4 + q) * 64 + c.lane) * 16) = f32x4{O[dt][4 * q], O[dt][4 * q + 1], O[dt][4 * q + 2], O[dt][4 * q + 3]};
    TS(16);
    __syncthreads();
    TS(17);
    f32x2 ml[8]; float M = -INFINITY;
#pragma unroll
    for (int k = 0; k < 8; k++) { ml[k] = *(f32x2*)(sm + L_ML + (k * 32 + (c.lane & 31)) * 8); M = fmaxf(M, ml[k][0]); }
    float L = 0.f; f32x4 o = {0.f, 0.f, 0.f, 0.f};
#pragma unroll
    for (int k = 0; k < 8; k++) {
      float f = __builtin_amdgcn_exp2f((ml[k][0] - M) * CS);
      L = fmaf(f, ml[k][1], L);
      o += f * *(f32x4*)(sm + L_COMB + ((k * 8 + c.w) * 64 + c.lane) * 16);
    }
    o *= __builtin_amdgcn_rcpf(L);
    __builtin_amdgcn_raw_buffer_store_b64(u32x2{san(pack2(o[0], o[1])), san(pack2(o[2], o[3]))}, rws, (c.w * 32 + (c.lane & 31)) * 16 + (c.lane >> 5) * 8, WS_ATT + (c.tg * 4 + c.s) * 4096, 16);
  }
  TS(18);
  u32x4 dwf[2][5], wp[16];
  {
    u32x4 wo[16], w1[8];
    load_tiled(wo, rws, c.lane * 16, wt + WT_WO + c.w * 16384);
    SB();
    u32x4 at[2];
    for (int sp = 0;; sp++) {
#pragma unroll
      for (int h = 0; h < 2; h++) at[h] = __builtin_amdgcn_raw_buffer_load_b128(rws, c.tid * 16 + h * 8192, WS_ATT + c.tg * 16384, 16);
      if (dead || !__any(max(umax4(at[0]), umax4(at[1])) == ~0u)) break;
      if (sp > SPIN_CAP) { dead = true; break; }
      asm volatile("" ::: "memory");
    }
    lds_w128(sm, L_ACT1 + c.tid * 16, at[0]); lds_w128(sm, L_ACT1 + c.tid * 16 + 8192, at[1]);
    TS(19);
#pragma unroll
    for (int q = 0; q < 8; q++) w1[q] = __builtin_amdgcn_raw_buffer_load_b128(rws, c.lane * 16 + (q & 3) * 1024, wt + WT_P1 + (c.s * 8 + c.w) * 8192 + (q >> 2) * 4096, 16);
    SB();
    __syncthreads();
    TS(20);
    zero_acc(acc);
    gemm_tiled(acc, wo, sm, L_ACT1, c);
#pragma unroll
    for (int th = 0; th < 2; th++) {
      float x[8], a[8]; unpack8(x, xr[th]); acc_row(a, acc, th);
#pragma unroll
      for (int e = 0; e < 8; e++) x[e] += a[e];
      xr[th] = pack8(x);
    }
    ln_to_act(xr, sm, L_ACT0, L_STAT, c);
    TS(21);
    f32x4 a2[2] = {f32x4{0.f, 0.f, 0.f, 0.f}, f32x4{0.f, 0.f, 0.f, 0.f}};
    const int base = L_ACT0 + (c.g * 32 + c.tok) * 16;
#pragma unroll
    for (int st = 0; st < 8; st++) {
      a2[0] = mfma16(lds_r128(sm, base + st * 2048), w1[st], a2[0]);
      a2[1] = mfma16(lds_r128(sm, base + st * 2048 + 256), w1[st], a2[1]);
    }
    const bool isA = c.tok < 8;
    const int th = c.tok >> 3;
    float gv[4];
#pragma unroll
    for (int j = 0; j < 4; j++) {
      float snd = isA ? a2[1][j] : a2[0][j];
      float rcv = __uint_as_float(__builtin_amdgcn_update_dpp(0, __float_as_uint(snd), 0x128, 0xf, 0xf, false));
      float a = isA ? a2[0][j] : rcv, b = isA ? rcv : a2[1][j];
      gv[j] = (c.tile * 32 + th * 16 + c.g * 4 + j < T_LEN) ? a * sigm_f(b) : 0.f;
    }
    __builtin_amdgcn_raw_buffer_store_b64(u32x2{san(pack2(gv[0], gv[1])), san(pack2(gv[2], gv[3]))}, rws, th * 8192 + (c.s * 64 + c.w * 8 + (c.tok & 7)) * 32 + c.g * 8, WS_G + (c.b * 64 + c.tile * 2) * 8192, 16);
  }
  TS(22);
  {
#pragma unroll
    for (int gq = 0; gq < 2; gq++)
#pragma unroll
      for (int m = 0; m < 5; m++) dwf[gq][m] = __builtin_amdgcn_raw_buffer_load_b128(rws, c.lane * 16, wt + WT_DW + ((c.w * 2 + gq) * 5 + m) * 1024, 16);
    SB();
    u32x4 gi[4];
    for (int sp = 0;; sp++) {
#pragma unroll
      for (int q = 0; q < 4; q++) {
        int ht = c.tile * 2 - 1 + q;
        gi[q] = __builtin_amdgcn_raw_buffer_load_b128(rws, (unsigned)ht < 64u ? WS_G + (c.b * 64 + ht) * 8192 + c.tid * 16 : 0x40000000, 0, 16);
      }
      if (dead || !__any(max(max(umax4(gi[0]), umax4(gi[1])), max(umax4(gi[2]), umax4(gi[3]))) == ~0u)) break;
      if (sp > SPIN_CAP) { dead = true; break; }
      asm volatile("" ::: "memory");
    }
#pragma unroll
    for (int q = 0; q < 4; q++) lds_w128(sm, L_COMB + (c.tid >> 1) * G_ROW + q * 32 + (c.tid & 1) * 16, gi[q]);
    TS(23);
    load_tiled(wp, rws, c.lane * 16, wt + WT_P2 + c.w * 16384);
    SB();
    __syncthreads();
    TS(24);
#pragma unroll
    for (int gq = 0; gq < 2; gq++) {
      const int slot = (c.w * 2 + gq) * 16 + (c.lane >> 2), n = c.lane & 3;
      u32x4 in[5];
#pragma unroll
      for (int m = 0; m < 5; m++) in[m] = lds_r128(sm, L_COMB + slot * G_ROW + n * 16 + m * 16);
      f32x4 d0 = {0.f, 0.f, 0.f, 0.f}, d1 = {0.f, 0.f, 0.f, 0.f};
#pragma unroll
      for (int j = 0; j < 9; j++) {
        u32x2 a = {dwf[gq][j >> 1][(j & 1) * 2], dwf[gq][j >> 1][(j & 1) * 2 + 1]};
        u32x2 b0 = {in[j >> 1][(j & 1) * 2], in[j >> 1][(j & 1) * 2 + 1]};
        u32x2 b1 = {in[(j + 1) >> 1][((j + 1) & 1) * 2], in[(j + 1) >> 1][((j + 1) & 1) * 2 + 1]};
        d0 = mfma4(a, b0, d0); d1 = mfma4(a, b1, d1);
      }
      u32x4 o = {pack2(silu_f(d0[0]), silu_f(d0[1])), pack2(silu_f(d0[2]), silu_f(d0[3])), pack2(silu_f(d1[0]), silu_f(d1[1])), pack2(silu_f(d1[2]), silu_f(d1[3]))};
      lds_w128(sm, L_ACT1 + slot * 64 + (n ^ (((slot >> 3) & 1) * 2)) * 16, o);
    }
    u32x4 wa[16], wb[16];
    SB();
    load_tiled(wa, rws, c.lane * 16, wt + WT_2A + (c.s * 8 + c.w) * 16384);
    SB();
    __syncthreads();
    TS(25);
    zero_acc(acc);
    {
      const int q = c.tok >> 2, pp = c.tok & 3;
      const int base = L_ACT1 + (c.g * 8 + q) * 64 + pp * 8;
#pragma unroll
      for (int st = 0; st < 8; st++) {
#pragma unroll
        for (int th = 0; th < 2; th++) {
          const int o = base + st * 2048 + ((th ^ (c.g & 1)) * 32);
          u32x2 lo = __builtin_bit_cast(u32x2, __builtin_amdgcn_ds_read_tr16_b64_v4bf16((LAS bf16x4*)(sm + o)));
          u32x2 hi = __builtin_bit_cast(u32x2, __builtin_amdgcn_ds_read_tr16_b64_v4bf16((LAS bf16x4*)(sm + o + 256)));
          u32x4 bf = {lo[0], lo[1], hi[0], hi[1]};
          acc[0][th] = mfma16(wp[st * 2], bf, acc[0][th]);
          acc[1][th] = mfma16(wp[st * 2 + 1], bf, acc[1][th]);
        }
      }
    }
    SB();
    load_tiled(wb, rws, c.lane * 16, wt + WT_2B + (c.s * 8 + c.w) * 16384);
    SB();
#pragma unroll
    for (int th = 0; th < 2; th++) {
      float x[8], a[8]; unpack8(x, xr[th]); acc_row(a, acc, th);
#pragma unroll
      for (int e = 0; e < 8; e++) x[e] += a[e];
      xr[th] = pack8(x);
    }
    ln_to_act(xr, sm, L_ACT0, L_STAT, c);
    TS(26);
    zero_acc(acc);
    gemm_tiled(acc, wa, sm, L_ACT0, c);
#pragma unroll
    for (int th = 0; th < 2; th++) { acc_row(v[th], acc, th);
#pragma unroll
      for (int e = 0; e < 8; e++) v[th][e] = silu_f(v[th][e]); }
    act_store(sm, L_ACT1, c, pack8(v[0]), pack8(v[1]));
    __syncthreads();
    TS(27);
    zero_acc(acc);
    gemm_tiled(acc, wb, sm, L_ACT1, c);
#pragma unroll
    for (int th = 0; th < 2; th++) { acc_row(v[th], acc, th);
      const int dest = th * 2 + (c.tok >> 3);
      if (dest != c.s) {
        u32x4 o = pack8(v[th]); o = u32x4{san(o[0]), san(o[1]), san(o[2]), san(o[3])};
        __builtin_amdgcn_raw_buffer_store_b128(o, rws, dest * 16384 + (c.tok & 7) * 512 + (c.w * 32 + c.g * 8) * 2, WS_P2 + (c.tg * 16 + c.s) * 4096, 16);
      } else {
        float x[8]; unpack8(x, xr[th]);
        const int o = L_ACT0 + (c.tok & 7) * 1040 + (c.w * 32 + c.g * 8) * 4;
        *(f32x4*)(sm + o) = f32x4{fmaf(v[th][0], 0.5f, x[0]), fmaf(v[th][1], 0.5f, x[1]), fmaf(v[th][2], 0.5f, x[2]), fmaf(v[th][3], 0.5f, x[3])};
        *(f32x4*)(sm + o + 16) = f32x4{fmaf(v[th][4], 0.5f, x[4]), fmaf(v[th][5], 0.5f, x[5]), fmaf(v[th][6], 0.5f, x[6]), fmaf(v[th][7], 0.5f, x[7])};
      }
    }
  }
  TS(28);
  __syncthreads();
  {
    f32x4 x = *(f32x4*)(sm + L_ACT0 + c.w * 1040 + c.lane * 16);
    u32x2 pp[4];
    for (int sp = 0;; sp++) {
#pragma unroll
      for (int s = 0; s < 4; s++) pp[s] = __builtin_amdgcn_raw_buffer_load_b64(rws, s == c.s ? 0x40000000 : c.w * 512 + c.lane * 8, WS_P2 + (c.tg * 16 + c.s * 4 + s) * 4096, 16);
      unsigned mx = 0;
#pragma unroll
      for (int s = 0; s < 4; s++) mx = max(mx, max(pp[s][0], pp[s][1]));
      if (dead || !__any(mx == ~0u)) break;
      if (sp > SPIN_CAP) { dead = true; break; }
      asm volatile("" ::: "memory");
    }
    TS(29);
    f32x4 a = {0.f, 0.f, 0.f, 0.f};
#pragma unroll
    for (int s = 0; s < 4; s++) a += f32x4{lo16(pp[s][0]), hi16(pp[s][0]), lo16(pp[s][1]), hi16(pp[s][1])};
    x += 0.5f * a;
    float mean = wave_sum(x[0] + x[1] + x[2] + x[3]) * (1.f / 256.f);
    x -= mean;
    float var = wave_sum(x[0] * x[0] + x[1] * x[1] + x[2] * x[2] + x[3] * x[3]) * (1.f / 256.f);
    float rstd = __builtin_amdgcn_rsqf(var + 1e-5f);
    const int t = c.tile * 32 + c.s * 8 + c.w;
    if (t < T_LEN) *(u32x2*)((char*)p.out + (size_t)(c.b * T_LEN + t) * 512 + c.lane * 8) = u32x2{pack2(x[0] * rstd, x[1] * rstd), pack2(x[2] * rstd, x[3] * rstd)};
  }
  TS(30); VMW(); TS(31); TS_OUT(0);
}

extern "C" void kernel_launch(void* const* d_in, const int* in_sizes, int n_in, void* d_out, int out_size, void* d_ws, size_t ws_size, hipStream_t stream) {
  Params p;
  p.x = (const u16*)d_in[0]; p.w1a = (const u16*)d_in[1]; p.w1b = (const u16*)d_in[2]; p.wqkv = (const u16*)d_in[3]; p.wo = (const u16*)d_in[4];
  p.wp1 = (const u16*)d_in[5]; p.dw = (const u16*)d_in[6]; p.wp2 = (const u16*)d_in[7]; p.w2a = (const u16*)d_in[8]; p.w2b = (const u16*)d_in[9];
  p.out = (u16*)d_out; p.ws = (char*)d_ws;
  k_all<<<256, 512, 0, stream>>>(p);
}
```

```cpp
#include <hip/hip_runtime.h>
#include <hip/hip_bf16.h>

typedef unsigned short u16;
typedef unsigned long long u64;
typedef __attribute__((ext_vector_type(2))) unsigned u32x2;
typedef __attribute__((ext_vector_type(4))) unsigned u32x4;
typedef __attribute__((ext_vector_type(2))) float f32x2;
typedef __attribute__((ext_vector_type(4))) float f32x4;
typedef __attribute__((ext_vector_type(16))) float f32x16;
typedef __attribute__((ext_vector_type(2))) __bf16 bf16x2;
typedef __attribute__((ext_vector_type(4))) __bf16 bf16x4;
typedef __attribute__((ext_vector_type(8))) __bf16 bf16x8;
typedef __attribute__((ext_vector_type(4))) short s16x4;
typedef __amdgpu_buffer_rsrc_t rsrc_t;
#define DI __device__ __forceinline__
#define LAS __attribute__((address_space(3)))
#define SB() __builtin_amdgcn_sched_barrier(0)

constexpr int T_LEN = 1000;
constexpr float LOG2E = 1.4426950408889634f;

constexpr unsigned WS_P1  = 0;
constexpr unsigned WS_KV  = WS_P1 + (4u << 20);
constexpr unsigned WS_Q   = WS_KV + (2u << 20);
constexpr unsigned WS_ATT = WS_Q + (1u << 20);
constexpr unsigned WS_G   = WS_ATT + (1u << 20);
constexpr unsigned WS_X   = WS_G + (1u << 20);
constexpr unsigned WS_P2  = WS_X + (3u << 20);
constexpr unsigned WS_WT  = WS_P2 + (4u << 20);
constexpr unsigned WT_P2 = 0, WT_2A = 128u << 10, WT_2B = 640u << 10, WT_P1 = 1152u << 10, WT_DW = 1408u << 10;
constexpr unsigned WS_SYNC = WS_WT + (4u << 20);
constexpr unsigned SY_E = 0, SY_F1 = 1024, SY_F2 = SY_F1 + 32768;
constexpr unsigned WS_END = WS_SYNC + (1u << 20);
constexpr unsigned WS_PROF = 64u << 20;

constexpr int L_RING = 0, L_ACT0 = 131072, L_ACT1 = 147456, L_END = 163840;
constexpr int L_STAT_A = L_ACT1, L_Q = L_ACT1 + 4096, L_COMB = L_ACT0, L_ML = 122880, L_STAT_B = 124928, L_G = L_ACT0;
#define BAR() asm volatile("s_waitcnt lgkmcnt(0)\n\ts_barrier" ::: "memory")
#define VMCNT(n) asm volatile("s_waitcnt vmcnt(" #n ")" ::: "memory")
#define ROT 1

DI rsrc_t mk_rsrc(const void* p, unsigned bytes) { return __builtin_amdgcn_make_buffer_rsrc((void*)p, 0, bytes, 0x00020000); }
DI unsigned pack2(float a, float b) { bf16x2 v = {(__bf16)a, (__bf16)b}; return __builtin_bit_cast(unsigned, v); }
DI float lo16(unsigned u) { return __uint_as_float(u << 16); }
DI float hi16(unsigned u) { return __uint_as_float(u & 0xffff0000u); }
DI f32x4 mfma16(u32x4 a, u32x4 b, f32x4 c) { return __builtin_amdgcn_mfma_f32_16x16x32_bf16(__builtin_bit_cast(bf16x8, a), __builtin_bit_cast(bf16x8, b), c, 0, 0, 0); }
DI f32x16 mfma32(u32x4 a, u32x4 b, f32x16 c) { return __builtin_amdgcn_mfma_f32_32x32x16_bf16(__builtin_bit_cast(bf16x8, a), __builtin_bit_cast(bf16x8, b), c, 0, 0, 0); }
DI f32x4 mfma4(u32x2 a, u32x2 b, f32x4 c) { return __builtin_amdgcn_mfma_f32_4x4x4bf16_1k(__builtin_bit_cast(s16x4, a), __builtin_bit_cast(s16x4, b), c, 0, 0, 0); }
DI float silu_f(float x) { return x * __builtin_amdgcn_rcpf(1.f + __builtin_amdgcn_exp2f(-LOG2E * x)); }
DI float sigm_f(float x) { return __builtin_amdgcn_rcpf(1.f + __builtin_amdgcn_exp2f(-LOG2E * x)); }
DI u32x4 lds_r128(char* sm, int off) { return *(u32x4*)(sm + off); }
DI void lds_w128(char* sm, int off, u32x4 v) { *(u32x4*)(sm + off) = v; }
DI float swadd32(float a, float b) { auto r = __builtin_amdgcn_permlane32_swap(__float_as_uint(a), __float_as_uint(b), false, false); return __uint_as_float(r[0]) + __uint_as_float(r[1]); }
DI float swadd16(float a, float b) { auto r = __builtin_amdgcn_permlane16_swap(__float_as_uint(a), __float_as_uint(b), false, false); return __uint_as_float(r[0]) + __uint_as_float(r[1]); }

struct Ctx { int tid, w, lane, tok, g, b, tile, s, tg; };
DI Ctx mk_ctx() {
  Ctx c; c.tid = threadIdx.x; c.w = __builtin_amdgcn_readfirstlane(threadIdx.x >> 6); c.lane = c.tid & 63; c.tok = c.lane & 15; c.g = c.lane >> 4;
  int bid = blockIdx.x; c.s = (bid >> 3) & 3; c.tg = (bid & 7) * 8 + (bid >> 5); c.b = c.tg >> 5; c.tile = c.tg & 31; return c;
}

DI u32x4 tr_frag(const unsigned* r8, int i) {
  const unsigned sel = i ? 0x07060302u : 0x05040100u;
  u32x4 f;
#pragma unroll
  for (int m = 0; m < 4; m++) f[m] = __builtin_amdgcn_perm(r8[2 * m + 1], r8[2 * m], sel);
  return f;
}
DI void load_tiled(u32x4 (&fr)[16], rsrc_t rs, int voff, int soff) {
#pragma unroll
  for (int q = 0; q < 16; q++) fr[q] = __builtin_amdgcn_raw_buffer_load_b128(rs, voff + (q & 3) * 1024, soff + (q >> 2) * 4096, 16);
}
DI void st128(u32x4 v, rsrc_t rs, unsigned voff, unsigned soff) { __builtin_amdgcn_raw_buffer_store_b128(v, rs, voff + soff, 0, 16); }
DI void dma16(rsrc_t rs, int voff, int soff, int lds) {
  asm volatile("s_mov_b32 m0, %0\n\ts_nop 0\n\tbuffer_load_dwordx4 %1, %2, %3 offen lds" :: "s"(lds), "v"(voff), "s"(rs), "s"(soff) : "memory");
}
DI void dma16s(rsrc_t rs, int voff, int soff, int lds) {
  asm volatile("s_mov_b32 m0, %0\n\ts_nop 0\n\tbuffer_load_dwordx4 %1, %2, %3 offen sc1 lds" :: "s"(lds), "v"(voff), "s"(rs), "s"(soff) : "memory");
}
DI u32x4 tr_pair(char* sm, int o0, int o1) {
  u32x2 lo = __builtin_bit_cast(u32x2, __builtin_amdgcn_ds_read_tr16_b64_v4bf16((LAS bf16x4*)(sm + o0)));
  u32x2 hi = __builtin_bit_cast(u32x2, __builtin_amdgcn_ds_read_tr16_b64_v4bf16((LAS bf16x4*)(sm + o1)));
  return u32x4{lo[0], lo[1], hi[0], hi[1]};
}
DI void gemm_tiled(f32x4 (&acc)[2][2], const u32x4 (&fr)[16], char* sm, int act, const Ctx& c) {
  const int base = act + (c.g * 32 + c.tok) * 16;
#pragma unroll
  for (int st = 0; st < 8; st++) {
    u32x4 b0 = lds_r128(sm, base + st * 2048), b1 = lds_r128(sm, base + st * 2048 + 256);
#pragma unroll
    for (int i = 0; i < 2; i++) {
      acc[i][0] = mfma16(fr[st * 2 + i], b0, acc[i][0]);
      acc[i][1] = mfma16(fr[st * 2 + i], b1, acc[i][1]);
    }
  }
}
DI void zero_acc(f32x4 (&acc)[2][2]) {
#pragma unroll
  for (int i = 0; i < 2; i++)
#pragma unroll
    for (int t = 0; t < 2; t++) acc[i][t] = f32x4{0.f, 0.f, 0.f, 0.f};
}
DI void unpack8(float (&v)[8], u32x4 p) {
#pragma unroll
  for (int m = 0; m < 4; m++) { v[2 * m] = lo16(p[m]); v[2 * m + 1] = hi16(p[m]); }
}
DI u32x4 pack8(const float (&v)[8]) { return u32x4{pack2(v[0], v[1]), pack2(v[2], v[3]), pack2(v[4], v[5]), pack2(v[6], v[7])}; }
DI void act_store(char* sm, int act, const Ctx& c, u32x4 p0, u32x4 p1) {
  const int o = act + ((c.w * 4 + c.g) * 32 + c.tok) * 16;
  lds_w128(sm, o, p0); lds_w128(sm, o + 256, p1);
}
DI void ln_part1(const u32x4 (&xr)[2], char* sm, int stat, const Ctx& c) {
  float v[2][8]; unpack8(v[0], xr[0]); unpack8(v[1], xr[1]);
  float s0 = 0, s1 = 0, q0 = 0, q1 = 0;
#pragma unroll
  for (int e = 0; e < 8; e++) { s0 += v[0][e]; s1 += v[1][e]; q0 = fmaf(v[0][e], v[0][e], q0); q1 = fmaf(v[1][e], v[1][e], q1); }
  float t = swadd16(swadd32(s0, s1), swadd32(q0, q1));
  *(float*)(sm + stat + (c.w * 64 + c.lane) * 4) = t;
}
DI void ln_part2(const u32x4 (&xr)[2], char* sm, int act, int stat, const Ctx& c) {
  float v[2][8]; unpack8(v[0], xr[0]); unpack8(v[1], xr[1]);
  float u = 0;
#pragma unroll
  for (int k = 0; k < 8; k++) u += *(float*)(sm + stat + (k * 64 + c.lane) * 4);
  auto r = __builtin_amdgcn_permlane16_swap(__float_as_uint(u), __float_as_uint(u), false, false);
  float mean = __uint_as_float(r[0]) * (1.f / 256.f);
  float var = fmaxf(fmaf(-mean, mean, __uint_as_float(r[1]) * (1.f / 256.f)), 0.f);
  float rstd = __builtin_amdgcn_rsqf(var + 1e-5f), nm = -mean * rstd;
  auto rr = __builtin_amdgcn_permlane32_swap(__float_as_uint(rstd), __float_as_uint(rstd), false, false);
  auto rn = __builtin_amdgcn_permlane32_swap(__float_as_uint(nm), __float_as_uint(nm), false, false);
#pragma unroll
  for (int t2 = 0; t2 < 2; t2++)
#pragma unroll
    for (int e = 0; e < 8; e++) v[t2][e] = fmaf(v[t2][e], __uint_as_float(rr[t2]), __uint_as_float(rn[t2]));
  act_store(sm, act, c, pack8(v[0]), pack8(v[1]));
}
DI void acc_row(float (&v)[8], const f32x4 (&acc)[2][2], int th) {
#pragma unroll
  for (int j = 0; j < 4; j++) { v[j] = acc[0][th][j]; v[4 + j] = acc[1][th][j]; }
}
struct Params { const u16 *x, *w1a, *w1b, *wqkv, *wo, *wp1, *dw, *wp2, *w2a, *w2b; u16* out; char* ws; };

DI unsigned ald16(rsrc_t rs, int voff) { unsigned r; asm volatile("buffer_load_ushort %0, %1, %2, 0 offen" : "=&v"(r) : "v"(voff), "s"(rs) : "memory"); return r; }
DI void prep_issue(const Params& p, int task, int lane, int lds, unsigned (&raw)[8]) {
  if (task < 352) {
    const int st = task & 7, r8 = lane >> 3, ck = lane & 7;
    const u16* src; int vo, so, step; unsigned bytes;
    if (task < 32) { src = p.wp2; bytes = 131072; vo = ((r8 >> 2) * 8 + (r8 & 3)) * 512 + ck * 16; so = st * 32 * 512 + (task >> 3) * 128; step = 0; }
    else if (task < 160) { src = p.w2a; bytes = 524288; vo = r8 * 2048 + ck * 16; so = st * 32 * 2048 + ((task - 32) >> 3) * 128; step = 8 * 2048; }
    else if (task < 288) { const int u = (task - 160) >> 3; src = p.w2b; bytes = 524288; vo = r8 * 512 + ck * 16; so = ((u >> 2) * 256 + st * 32) * 512 + (u & 3) * 128; step = 8 * 512; }
    else { src = p.wp1; bytes = 262144; vo = r8 * 1024 + (ck >> 1) * 16 + (ck & 1) * 512; so = st * 32 * 1024 + ((task - 288) >> 3) * 64; step = 8 * 1024; }
    rsrc_t rs = mk_rsrc(src, bytes);
#pragma unroll
    for (int i = 0; i < 4; i++) dma16(rs, vo + so + (task < 32 ? ((i & 1) * 16 + (i >> 1) * 4) * 512 : i * step), 0, lds + i * 1024);
  } else {
    const int t = task - 352, grp = t / 5, m = t - grp * 5;
    const int ch = (16 * grp + (lane >> 2)) & 255, i = lane & 3;
    rsrc_t rs = mk_rsrc(p.dw, 31 * 512);
#pragma unroll
    for (int e = 0; e < 8; e++) raw[e] = ald16(rs, (unsigned)(8 * m + e - 1 - i) < 31u ? (8 * m + e - 1 - i) * 512 + ch * 2 : 0x100000);
  }
}
DI void prep_finish(int task, int lane, char* sm, int lds, const unsigned (&raw)[8], rsrc_t rws) {
  if (task < 352) {
    const int st = task & 7, tb = lds + ((lane >> 4) * 4 + ((lane & 15) >> 2)) * 128 + (lane & 3) * 8;
    const unsigned d = task < 288 ? WS_WT + WT_P2 + (task >> 3) * 32768 + st * 2048 : WS_WT + WT_P1 + ((task - 288) >> 3) * 32768 + st * 1024;
#pragma unroll
    for (int i = 0; i < 4; i++) st128(tr_pair(sm, tb + i * 32, tb + i * 32 + 2048), rws, lane * 16, d + (task < 288 ? (i >> 1) * 16384 + (i & 1) * 1024 : i * 8192));
  } else st128(tr_frag(raw, 0), rws, lane * 16, task < 432 ? WS_WT + WT_DW + (task - 352) * 1024 : 0x40000000u);
}

#define TS_DECL
#define TS(i)
#define TS_OUT(k)
#define VMW()
#define KATTR __global__ __launch_bounds__(512) __attribute__((amdgpu_waves_per_eu(2, 2)))

DI float wave_sum(float v) {
  v += __uint_as_float(__builtin_amdgcn_update_dpp(0, __float_as_uint(v), 0xB1, 0xf, 0xf, false));
  v += __uint_as_float(__builtin_amdgcn_update_dpp(0, __float_as_uint(v), 0x4E, 0xf, 0xf, false));
  v += __uint_as_float(__builtin_amdgcn_update_dpp(0, __float_as_uint(v), 0x124, 0xf, 0xf, false));
  v += __uint_as_float(__builtin_amdgcn_update_dpp(0, __float_as_uint(v), 0x128, 0xf, 0xf, false));
  v = swadd16(v, v); v = swadd32(v, v);
  return v;
}

constexpr int SPIN_CAP = 1 << 17;
#define DRAIN() asm volatile("s_waitcnt vmcnt(0)" ::: "memory")
DI u32x2 ld_flag(rsrc_t rws, int off) { return __builtin_amdgcn_raw_buffer_load_b64(rws, off, WS_SYNC, 16); }
DI void st_flag(rsrc_t rws, int off, u32x2 v) { __builtin_amdgcn_raw_buffer_store_b64(v, rws, off, WS_SYNC, 16); }
DI void poll_flags(rsrc_t rws, int off, u32x2 ep, bool& dead) {
  if (dead) return;
  for (int sp = 0;; sp++) {
    u32x2 f = ld_flag(rws, off);
    if (__all(f[0] == ep[0] && f[1] == ep[1])) break;
    if (sp > SPIN_CAP) { dead = true; break; }
    asm volatile("" ::: "memory");
  }
  asm volatile("" ::: "memory");
}
DI unsigned umax4(u32x4 v) { return max(max(v[0], v[1]), max(v[2], v[3])); }
DI unsigned san(unsigned v) { return min(v, 0xFFFFFFFEu); }

KATTR void k_all(Params p) {
  __shared__ __attribute__((aligned(16))) char sm[L_END];
  TS_DECL; TS(0);
  const Ctx c = mk_ctx();
  rsrc_t rws = mk_rsrc(p.ws, WS_END);
  const unsigned wt = WS_WT;
  bool dead = false;
  u32x2 ep;
  asm volatile("s_nop 4\n\tbuffer_load_dwordx2 %0, %1, %2, 0 offen sc1" : "=&v"(ep) : "v"(WS_SYNC + SY_E), "s"(rws) : "memory");
  u32x4 xr[2];
  f32x4 acc[2][2];
  float v[2][8];
  const int bbase = (c.g * 32 + c.tok) * 16;
    const int rot = ROT ? (c.tg & 3) : 0;
    rsrc_t rx = mk_rsrc(p.x, 2000 * 512), ra = mk_rsrc(p.w1a, 524288), rb = mk_rsrc(p.w1b, 524288), rq = mk_rsrc(p.wqkv, 393216), ro = mk_rsrc(p.wo, 131072);
    u32x2 xl[4];
#pragma unroll
    for (int th = 0; th < 2; th++) {
      const int t = c.tile * 32 + th * 16 + c.tok;
      const int off = t < T_LEN ? (c.b * T_LEN + t) * 512 + (c.w * 32 + c.g * 4) * 2 : 0x10000000;
      asm volatile("s_nop 4\n\tbuffer_load_dwordx2 %0, %2, %3, 0 offen\n\tbuffer_load_dwordx2 %1, %2, %3, 0 offen offset:32" : "=&v"(xl[2 * th]), "=&v"(xl[2 * th + 1]) : "v"(off), "s"(rx) : "memory");
    }
    int va[4], vb[4];
#pragma unroll
    for (int i = 0; i < 4; i++) { const int row = 2 * i + (c.lane >> 5), ch = (c.lane & 31) ^ (row << 1); va[i] = row * 2048 + ch * 16; vb[i] = row * 512 + ch * 16; }
    const int vq = (c.lane >> 3) * 1536 + (((c.lane & 7) ^ (((c.lane >> 4) & 3) << 1)) * 16);
    auto dma_a = [&](int k) { const int kc = (k + rot) & 3;
#pragma unroll
      for (int i = 0; i < 4; i++) dma16(ra, va[i], (kc * 64 + c.w * 8) * 2048 + c.s * 512, L_RING + k * 32768 + c.w * 4096 + i * 1024); };
    auto dma_b = [&](int k) { const int kc = (k + rot) & 3;
#pragma unroll
      for (int i = 0; i < 4; i++) dma16(rb, vb[i], (c.s * 256 + kc * 64 + c.w * 8) * 512, L_RING + k * 32768 + c.w * 4096 + i * 1024); };
    auto dma_q = [&](int seg) {
#pragma unroll
      for (int i = 0; i < 4; i++) dma16(rq, vq, (c.w * 32 + i * 8) * 1536 + seg * 512 + c.s * 128, L_RING + seg * 32768 + c.w * 4096 + i * 1024); };
    const int trow = c.g * 4 + (c.tok >> 2), tp = c.tok & 3;
    const int tb0 = L_RING + trow * 512 + (((c.w * 4 + (tp >> 1)) ^ ((trow & 7) << 1)) * 16) + (tp & 1) * 8, tb1 = tb0 ^ 32;
    u32x4 fr[16];
    auto tr_chunk = [&](int k) {
#pragma unroll
      for (int h = 0; h < 2; h++) { const int o = k * 32768 + h * 16384;
        fr[(2 * k + h) * 2] = tr_pair(sm, tb0 + o, tb0 + o + 8192); fr[(2 * k + h) * 2 + 1] = tr_pair(sm, tb1 + o, tb1 + o + 8192); } };
    u32x4 bb[8];
    auto act_rd = [&](int k2, int act) {
#pragma unroll
      for (int k = 0; k < 2; k++) { const int o = act + bbase + ((k2 + k + rot) & 3) * 4096;
#pragma unroll
        for (int h = 0; h < 2; h++) { bb[k * 4 + h * 2] = lds_r128(sm, o + h * 2048); bb[k * 4 + h * 2 + 1] = lds_r128(sm, o + h * 2048 + 256); } } };
    auto mm2 = [&](int k2) {
#pragma unroll
      for (int q = 0; q < 4; q++)
#pragma unroll
        for (int i = 0; i < 2; i++) { acc[i][0] = mfma16(fr[(2 * k2 + q) * 2 + i], bb[2 * q], acc[i][0]); acc[i][1] = mfma16(fr[(2 * k2 + q) * 2 + i], bb[2 * q + 1], acc[i][1]); } };
    dma_a(0); dma_a(1);
    TS(1);
    asm volatile("s_waitcnt vmcnt(8)" : "+v"(xl[0]), "+v"(xl[1]), "+v"(xl[2]), "+v"(xl[3]), "+v"(ep) :: "memory");
    { u64 e = (((u64)ep[1] << 32) | ep[0]) + 1; ep[0] = __builtin_amdgcn_readfirstlane((unsigned)e); ep[1] = __builtin_amdgcn_readfirstlane((unsigned)(e >> 32)); }
    xr[0] = u32x4{xl[0][0], xl[0][1], xl[1][0], xl[1][1]}; xr[1] = u32x4{xl[2][0], xl[2][1], xl[3][0], xl[3][1]};
    ln_part1(xr, sm, L_STAT_A, c);
    dma_a(2);
    BAR();
    ln_part2(xr, sm, L_ACT0, L_STAT_A, c);
    dma_a(3);
    VMCNT(8); BAR();
    TS(2);
    tr_chunk(0); tr_chunk(1); act_rd(0, L_ACT0); zero_acc(acc); mm2(0);
    VMCNT(0); BAR();
    tr_chunk(2); tr_chunk(3); act_rd(2, L_ACT0);
    dma_b(0); dma_b(1);
    BAR();
    mm2(2); SB();
    dma_b(2); dma_b(3);
#pragma unroll
    for (int th = 0; th < 2; th++) { acc_row(v[th], acc, th);
#pragma unroll
      for (int e = 0; e < 8; e++) v[th][e] = silu_f(v[th][e]); }
    act_store(sm, L_ACT1, c, pack8(v[0]), pack8(v[1]));
    VMCNT(8); BAR();
    TS(3);
    tr_chunk(0); tr_chunk(1); act_rd(0, L_ACT1); zero_acc(acc); mm2(0);
    VMCNT(0); BAR();
    tr_chunk(2); tr_chunk(3); act_rd(2, L_ACT1); mm2(2);
  u32x4 own[2];
#pragma unroll
  for (int th = 0; th < 2; th++) { acc_row(v[th], acc, th); own[th] = pack8(v[th]);
    st128(own[th], rws, c.tid * 16 + th * 8192, WS_P1 + (c.tg * 4 + c.s) * 16384); }
  TS(4);
  dma_q(0); dma_q(1);
  VMCNT(8);
  BAR();
  if (c.w == 0) st_flag(rws, c.lane ? 0x40000000 : SY_F1 + (c.tg * 4 + c.s) * 128, ep);
  dma_q(2);
  unsigned raw[8];
  const int task = (c.w - 6) * 256 + blockIdx.x, plds = L_RING + 3 * 32768 + (c.w & 1) * 4096;
  if (c.w >= 6) prep_issue(p, task, c.lane, plds, raw);
  TS(5);
  {
    poll_flags(rws, SY_F1 + (c.tg * 4 + (c.lane & 3)) * 128, ep, dead);
    TS(7);
    u32x4 pp[3][2];
#pragma unroll
    for (int k = 0; k < 3; k++)
#pragma unroll
      for (int th = 0; th < 2; th++) asm volatile("buffer_load_dwordx4 %0, %1, %2, %3 offen sc1" : "=&v"(pp[k][th]) : "v"(c.tid * 16 + th * 8192), "s"(rws), "s"(WS_P1 + (c.tg * 4 + ((c.s + 1 + k) & 3)) * 16384) : "memory");
    if (c.w >= 6) {
      asm volatile("" : "+v"(raw[0]), "+v"(raw[1]), "+v"(raw[2]), "+v"(raw[3]), "+v"(raw[4]), "+v"(raw[5]), "+v"(raw[6]), "+v"(raw[7]) :: "memory");
      prep_finish(task, c.lane, sm, plds, raw, rws);
      if (task < 352) VMCNT(4); else VMCNT(1);
    } else VMCNT(0);
    asm volatile("" : "+v"(pp[0][0]), "+v"(pp[0][1]), "+v"(pp[1][0]), "+v"(pp[1][1]), "+v"(pp[2][0]), "+v"(pp[2][1]) :: "memory");
    SB(); TS(8);
#pragma unroll
    for (int th = 0; th < 2; th++) {
      float x[8], a[8], t[8]; unpack8(x, xr[th]); unpack8(a, own[th]);
#pragma unroll
      for (int k = 0; k < 3; k++) { unpack8(t, pp[k][th]);
#pragma unroll
        for (int e = 0; e < 8; e++) a[e] += t[e]; }
#pragma unroll
      for (int e = 0; e < 8; e++) x[e] = fmaf(a[e], 0.5f, x[e]);
      xr[th] = pack8(x);
    }
    TS(9);
    ln_part1(xr, sm, L_STAT_A, c);
    {
      const u32x4 ff = {~0u, ~0u, ~0u, ~0u};
      st128(ff, rws, c.lane * 16, c.w < 4 ? WS_ATT + (c.tg * 4 + c.s) * 4096 + c.w * 1024 : WS_G + (c.b * 64 + c.tile * 2 + ((c.w >> 1) & 1)) * 8192 + c.s * 2048 + (c.w & 1) * 1024);
    }
    BAR();
    dma16(ro, ((c.w & 1) ? vb[2] : vb[0]), (28 + (c.w >> 1)) * 4096, L_RING + (28 + (c.w >> 1)) * 4096 + (c.w & 1) * 2048);
    dma16(ro, ((c.w & 1) ? vb[3] : vb[1]), (28 + (c.w >> 1)) * 4096, L_RING + (28 + (c.w >> 1)) * 4096 + (c.w & 1) * 2048 + 1024);
    u32x4 wq[16];
    {
      const int ws_ = c.w < 6 ? c.w : 0;
      const int trow = c.g * 4 + (c.tok >> 2), tp = c.tok & 3;
      const int tb0 = L_RING + (ws_ >> 1) * 32768 + trow * 128 + ((((ws_ & 1) * 4 + (tp >> 1)) ^ (((trow >> 1) & 3) << 1)) * 16) + (tp & 1) * 8, tb1 = tb0 ^ 32;
#pragma unroll
      for (int st = 0; st < 8; st++) { wq[2 * st] = tr_pair(sm, tb0 + st * 4096, tb0 + st * 4096 + 2048); wq[2 * st + 1] = tr_pair(sm, tb1 + st * 4096, tb1 + st * 4096 + 2048); }
    }
    ln_part2(xr, sm, L_ACT0, L_STAT_A, c); BAR();
    TS(10);
    zero_acc(acc);
    const unsigned kvb = WS_KV + ((c.b * 4 + c.s) * 32 + c.tile) * 8192;
    if (c.w < 4) {
      gemm_tiled(acc, wq, sm, L_ACT0, c);
#pragma unroll
      for (int th = 0; th < 2; th++) { acc_row(v[th], acc, th);
        const int o = (((c.w & 1) * 4 + c.g) * 32 + th * 16 + c.tok) * 16;
        if (c.w < 2) lds_w128(sm, L_Q + o, pack8(v[th]));
        else st128(pack8(v[th]), rws, o, kvb); }
    } else if (c.w < 6) {
#pragma unroll
      for (int st = 0; st < 8; st++) {
        u32x4 b0 = lds_r128(sm, L_ACT0 + bbase + st * 2048), b1 = lds_r128(sm, L_ACT0 + bbase + st * 2048 + 256);
#pragma unroll
        for (int i = 0; i < 2; i++) { acc[i][0] = mfma16(b0, wq[st * 2 + i], acc[i][0]); acc[i][1] = mfma16(b1, wq[st * 2 + i], acc[i][1]); }
      }
#pragma unroll
      for (int th = 0; th < 2; th++) {
        auto r0 = __builtin_amdgcn_permlane32_swap(pack2(acc[0][th][0], acc[0][th][1]), pack2(acc[1][th][0], acc[1][th][1]), false, false);
        auto r1 = __builtin_amdgcn_permlane32_swap(pack2(acc[0][th][2], acc[0][th][3]), pack2(acc[1][th][2], acc[1][th][3]), false, false);
        u32x4 o = {r0[0], r1[0], r0[1], r1[1]};
        st128(o, rws, (8 * (c.tok >> 2) + 4 * (c.lane >> 5) + (c.tok & 3) + 32 * (c.g & 1)) * 16 + ((c.w & 1) * 2 + th) * 1024, kvb + 4096);
      }
    }
  }
  TS(11);
  DRAIN();
  BAR();
  if (c.w == 0) st_flag(rws, c.lane ? 0x40000000 : SY_F2 + (c.tg * 4 + c.s) * 128, ep);
  TS(12);
  u32x4 wo[16];
  {
    u32x4 qf[4], kf[4][4], vf[4][4];
#pragma unroll
    for (int ks = 0; ks < 4; ks++) qf[ks] = lds_r128(sm, L_Q + c.lane * 16 + ks * 1024);
    wo[14] = tr_pair(sm, tb0 + 7 * 16384, tb0 + 7 * 16384 + 8192); wo[15] = tr_pair(sm, tb1 + 7 * 16384, tb1 + 7 * 16384 + 8192);
    BAR();
    if (c.w) {
#pragma unroll
      for (int n = 0; n < 16; n++) dma16(ro, vb[n & 3], ((c.w - 1) * 4 + (n >> 2)) * 4096, L_RING + (c.w - 1) * 16384 + n * 1024);
    }
    if (c.w < 6) {
      const u32x4 ff = {~0u, ~0u, ~0u, ~0u};
      const unsigned o = WS_P2 + (c.tg * 16 + c.s) * 4096 + ((c.s + 1 + (c.w >> 1)) & 3) * 16384 + (c.w & 1) * 2048;
      st128(ff, rws, c.lane * 16, o); st128(ff, rws, c.lane * 16 + 1024, o);
    }
    poll_flags(rws, SY_F2 + ((((c.lane >> 4) & 1) * 32 + c.w * 4) * 4 + (c.lane & 15)) * 128, ep, dead);
    TS(14);
    const unsigned kvb = WS_KV + ((c.b * 4 + c.s) * 32 + c.w * 4) * 8192;
    __builtin_amdgcn_s_setprio(2);
#pragma unroll
    for (int t = 0; t < 4; t++)
#pragma unroll
      for (int ks = 0; ks < 4; ks++) kf[t][ks] = __builtin_amdgcn_raw_buffer_load_b128(rws, c.lane * 16 + ks * 1024, kvb + t * 8192, 16);
    SB();
    __builtin_amdgcn_s_setprio(1);
    TS(13);
    f32x16 S[4];
#pragma unroll
    for (int t = 0; t < 4; t++) {
#pragma unroll
      for (int e = 0; e < 16; e++) S[t][e] = 0.f;
#pragma unroll
      for (int ks = 0; ks < 4; ks++) S[t] = mfma32(kf[t][ks], qf[ks], S[t]);
      SB();
#pragma unroll
      for (int ks = 0; ks < 4; ks++) vf[t][ks] = __builtin_amdgcn_raw_buffer_load_b128(rws, c.lane * 16 + ks * 1024, kvb + t * 8192 + 4096, 16);
      SB();
    }
    __builtin_amdgcn_s_setprio(0);
    TS(15);
    if (c.w == 7) {
#pragma unroll
      for (int e = 4; e < 16; e++) S[3][e] = -INFINITY;
    }
    float m = S[0][0];
#pragma unroll
    for (int t = 0; t < 4; t++)
#pragma unroll
      for (int e = 0; e < 16; e++) m = fmaxf(m, S[t][e]);
    { auto r = __builtin_amdgcn_permlane32_swap(__float_as_uint(m), __float_as_uint(m), false, false); m = fmaxf(__uint_as_float(r[0]), __uint_as_float(r[1])); }
    const float CS = 0.125f * LOG2E;
    const float mc = -m * CS;
    float l = 0.f;
    f32x16 O[2];
#pragma unroll
    for (int e = 0; e < 16; e++) { O[0][e] = 0.f; O[1][e] = 0.f; }
#pragma unroll
    for (int t = 0; t < 4; t++) {
#pragma unroll
      for (int e = 0; e < 16; e++) { S[t][e] = __builtin_amdgcn_exp2f(fmaf(S[t][e], CS, mc)); l += S[t][e]; }
#pragma unroll
      for (int s2 = 0; s2 < 2; s2++) {
        u32x4 pf = {pack2(S[t][8 * s2], S[t][8 * s2 + 1]), pack2(S[t][8 * s2 + 2], S[t][8 * s2 + 3]), pack2(S[t][8 * s2 + 4], S[t][8 * s2 + 5]), pack2(S[t][8 * s2 + 6], S[t][8 * s2 + 7])};
        O[0] = mfma32(vf[t][s2], pf, O[0]);
        O[1] = mfma32(vf[t][2 + s2], pf, O[1]);
      }
    }
    l = swadd32(l, l);
    if (c.lane < 32) *(f32x2*)(sm + L_ML + (c.w * 32 + c.lane) * 8) = f32x2{m, l};
    const float rl = __builtin_amdgcn_rcpf(l);
#pragma unroll
    for (int dt = 0; dt < 2; dt++)
#pragma unroll
      for (int q = 0; q < 4; q++) *(u32x2*)(sm + L_COMB + ((c.w * 8 + dt * 4 + q) * 64 + c.lane) * 8) = u32x2{pack2(O[dt][4 * q] * rl, O[dt][4 * q + 1] * rl), pack2(O[dt][4 * q + 2] * rl, O[dt][4 * q + 3] * rl)};
    TS(16);
    BAR();
    if (c.w == 0 && blockIdx.x == 0) st_flag(rws, c.lane ? 0x40000000 : SY_E, ep);
    TS(17);
    f32x2 ml[8]; u32x2 ok[8]; float M = -INFINITY;
#pragma unroll
    for (int k = 0; k < 8; k++) ml[k] = *(f32x2*)(sm + L_ML + (k * 32 + (c.lane & 31)) * 8);
#pragma unroll
    for (int k = 0; k < 8; k++) ok[k] = *(u32x2*)(sm + L_COMB + ((k * 8 + c.w) * 64 + c.lane) * 8);
    SB();
#pragma unroll
    for (int st = 0; st < 7; st++) { wo[2 * st] = tr_pair(sm, tb0 + st * 16384, tb0 + st * 16384 + 8192); wo[2 * st + 1] = tr_pair(sm, tb1 + st * 16384, tb1 + st * 16384 + 8192); }
    SB();
#pragma unroll
    for (int k = 0; k < 8; k++) M = fmaxf(M, ml[k][0]);
    float L = 0.f; f32x4 o = {0.f, 0.f, 0.f, 0.f};
#pragma unroll
    for (int k = 0; k < 8; k++) {
      const float f = __builtin_amdgcn_exp2f((ml[k][0] - M) * CS) * ml[k][1];
      L += f;
      o += f * f32x4{lo16(ok[k][0]), hi16(ok[k][0]), lo16(ok[k][1]), hi16(ok[k][1])};
    }
    o *= __builtin_amdgcn_rcpf(L);
    __builtin_amdgcn_raw_buffer_store_b64(u32x2{san(pack2(o[0], o[1])), san(pack2(o[2], o[3]))}, rws, (c.w * 32 + (c.lane & 31)) * 16 + (c.lane >> 5) * 8, WS_ATT + (c.tg * 4 + c.s) * 4096, 16);
  }
  BAR();
  TS(18);
  u32x4 dwf[2][5], wp[16];
  {
    u32x4 w1[8];
#pragma unroll
    for (int q = 0; q < 8; q++) w1[q] = __builtin_amdgcn_raw_buffer_load_b128(rws, c.lane * 16 + (q & 3) * 1024, wt + WT_P1 + (c.s * 8 + c.w) * 8192 + (q >> 2) * 4096, 16);
    SB();
#pragma unroll
    for (int q = 0; q < 8; q++) dma16s(rws, c.lane * 16, wt + WT_2A + (c.s * 8 + c.w) * 16384 + q * 1024, L_RING + c.w * 15360 + q * 1024);
    u32x4 at[2];
    for (int sp = 0;; sp++) {
#pragma unroll
      for (int h = 0; h < 2; h++) at[h] = __builtin_amdgcn_raw_buffer_load_b128(rws, c.tid * 16 + h * 8192, WS_ATT + c.tg * 16384, 16);
      if (dead || !__any(max(umax4(at[0]), umax4(at[1])) == ~0u)) break;
      if (sp > SPIN_CAP) { dead = true; break; }
      asm volatile("" ::: "memory");
    }
    lds_w128(sm, L_ACT1 + c.tid * 16, at[0]); lds_w128(sm, L_ACT1 + c.tid * 16 + 8192, at[1]);
    TS(19);
    BAR();
    TS(20);
    zero_acc(acc);
    {
      u32x4 bq[16];
#pragma unroll
      for (int st = 0; st < 8; st++) { bq[2 * st] = lds_r128(sm, L_ACT1 + bbase + st * 2048); bq[2 * st + 1] = lds_r128(sm, L_ACT1 + bbase + st * 2048 + 256); }
#pragma unroll
      for (int st = 0; st < 8; st++) {
        if (st < 7) dma16s(rws, c.lane * 16, wt + WT_2A + (c.s * 8 + c.w) * 16384 + (8 + st) * 1024, L_RING + c.w * 15360 + (8 + st) * 1024);
#pragma unroll
        for (int i = 0; i < 2; i++) { acc[i][0] = mfma16(wo[st * 2 + i], bq[2 * st], acc[i][0]); acc[i][1] = mfma16(wo[st * 2 + i], bq[2 * st + 1], acc[i][1]); }
        SB();
      }
    }
#pragma unroll
    for (int th = 0; th < 2; th++) {
      float x[8], a[8]; unpack8(x, xr[th]); acc_row(a, acc, th);
#pragma unroll
      for (int e = 0; e < 8; e++) x[e] += a[e];
      xr[th] = pack8(x);
    }
    ln_part1(xr, sm, L_STAT_B, c);
#pragma unroll
    for (int m = 0; m < 5; m++) dwf[0][m] = __builtin_amdgcn_raw_buffer_load_b128(rws, c.lane * 16, wt + WT_DW + (c.w * 10 + m) * 1024, 16);
    BAR(); ln_part2(xr, sm, L_ACT0, L_STAT_B, c);
#pragma unroll
    for (int m = 0; m < 5; m++) dwf[1][m] = __builtin_amdgcn_raw_buffer_load_b128(rws, c.lane * 16, wt + WT_DW + (c.w * 10 + 5 + m) * 1024, 16);
    BAR();
    TS(21);
    f32x4 a2[2] = {f32x4{0.f, 0.f, 0.f, 0.f}, f32x4{0.f, 0.f, 0.f, 0.f}};
    const int base = L_ACT0 + (c.g * 32 + c.tok) * 16;
#pragma unroll
    for (int st = 0; st < 8; st++) {
      a2[0] = mfma16(lds_r128(sm, base + st * 2048), w1[st], a2[0]);
      a2[1] = mfma16(lds_r128(sm, base + st * 2048 + 256), w1[st], a2[1]);
    }
    const bool isA = c.tok < 8;
    const int th = c.tok >> 3;
    float gv[4];
#pragma unroll
    for (int j = 0; j < 4; j++) {
      float snd = isA ? a2[1][j] : a2[0][j];
      float rcv = __uint_as_float(__builtin_amdgcn_update_dpp(0, __float_as_uint(snd), 0x128, 0xf, 0xf, false));
      float a = isA ? a2[0][j] : rcv, b = isA ? rcv : a2[1][j];
      gv[j] = (c.tile * 32 + th * 16 + c.g * 4 + j < T_LEN) ? a * sigm_f(b) : 0.f;
    }
    __builtin_amdgcn_raw_buffer_store_b64(u32x2{san(pack2(gv[0], gv[1])), san(pack2(gv[2], gv[3]))}, rws, th * 8192 + (c.s * 64 + c.w * 8 + (c.tok & 7)) * 32 + c.g * 8, WS_G + (c.b * 64 + c.tile * 2) * 8192, 16);
  }
  BAR();
  TS(22);
  {
    load_tiled(wp, rws, c.lane * 16, wt + WT_P2 + c.w * 16384);
    SB();
    u32x4 gi[4];
    for (int sp = 0;; sp++) {
#pragma unroll
      for (int q = 0; q < 4; q++) {
        int ht = c.tile * 2 - 1 + q;
        gi[q] = __builtin_amdgcn_raw_buffer_load_b128(rws, (unsigned)ht < 64u ? WS_G + (c.b * 64 + ht) * 8192 + c.tid * 16 : 0x40000000, 0, 16);
      }
      if (dead || !__any(max(max(umax4(gi[0]), umax4(gi[1])), max(umax4(gi[2]), umax4(gi[3]))) == ~0u)) break;
      if (sp > SPIN_CAP) { dead = true; break; }
      asm volatile("" ::: "memory");
    }
#pragma unroll
    for (int q = 0; q < 4; q++) lds_w128(sm, L_G + (c.tid >> 1) * 128 + (((q * 2 + (c.tid & 1)) ^ (c.tid & 4)) * 16), gi[q]);
    TS(23);
    BAR();
    TS(24);
    u32x4 wb[16], wa15;
    const unsigned wbo = wt + WT_2B + (c.s * 8 + c.w) * 16384;
#pragma unroll
    for (int gq = 0; gq < 2; gq++) {
      const int slot = (c.w * 2 + gq) * 16 + (c.lane >> 2), n = c.lane & 3;
      u32x4 in[5];
#pragma unroll
      for (int m = 0; m < 5; m++) in[m] = lds_r128(sm, L_G + slot * 128 + (((n + m) ^ ((slot & 2) << 1)) * 16));
      SB();
#pragma unroll
      for (int q = 0; q < 2; q++) wb[gq * 4 + q] = __builtin_amdgcn_raw_buffer_load_b128(rws, c.lane * 16 + q * 1024, wbo + gq * 4096, 16);
      SB();
      f32x4 d0 = {0.f, 0.f, 0.f, 0.f}, d1 = {0.f, 0.f, 0.f, 0.f};
#pragma unroll
      for (int j = 0; j < 9; j++) {
        u32x2 a = {dwf[gq][j >> 1][(j & 1) * 2], dwf[gq][j >> 1][(j & 1) * 2 + 1]};
        u32x2 b0 = {in[j >> 1][(j & 1) * 2], in[j >> 1][(j & 1) * 2 + 1]};
        u32x2 b1 = {in[(j + 1) >> 1][((j + 1) & 1) * 2], in[(j + 1) >> 1][((j + 1) & 1) * 2 + 1]};
        d0 = mfma4(a, b0, d0); d1 = mfma4(a, b1, d1);
      }
      SB();
#pragma unroll
      for (int q = 2; q < 4; q++) wb[gq * 4 + q] = __builtin_amdgcn_raw_buffer_load_b128(rws, c.lane * 16 + q * 1024, wbo + gq * 4096, 16);
      SB();
      u32x4 o = {pack2(silu_f(d0[0]), silu_f(d0[1])), pack2(silu_f(d0[2]), silu_f(d0[3])), pack2(silu_f(d1[0]), silu_f(d1[1])), pack2(silu_f(d1[2]), silu_f(d1[3]))};
      lds_w128(sm, L_G + slot * 128 + (slot & 2) * 32 + (n ^ (((slot >> 3) & 1) * 2)) * 16, o);
    }
    BAR();
    TS(25);
    zero_acc(acc);
    {
      const int q = c.tok >> 2, pp = c.tok & 3;
      const int base = L_G + (c.g * 8 + q) * 128 + (q & 2) * 32 + pp * 8;
      u32x4 bq[16];
#pragma unroll
      for (int st = 0; st < 8; st++)
#pragma unroll
        for (int th = 0; th < 2; th++) { const int o = base + st * 4096 + ((th ^ (c.g & 1)) * 32); bq[2 * st + th] = tr_pair(sm, o, o + 512); }
      SB();
      wa15 = __builtin_amdgcn_raw_buffer_load_b128(rws, c.lane * 16, wt + WT_2A + (c.s * 8 + c.w) * 16384 + 15360, 16);
#pragma unroll
      for (int st = 0; st < 8; st++) {
        wb[8 + st] = __builtin_amdgcn_raw_buffer_load_b128(rws, c.lane * 16 + (st & 3) * 1024, wbo + 8192 + (st >> 2) * 4096, 16);
#pragma unroll
        for (int th = 0; th < 2; th++) { acc[0][th] = mfma16(wp[st * 2], bq[2 * st + th], acc[0][th]); acc[1][th] = mfma16(wp[st * 2 + 1], bq[2 * st + th], acc[1][th]); }
        SB();
      }
    }
#pragma unroll
    for (int th = 0; th < 2; th++) {
      float x[8], a[8]; unpack8(x, xr[th]); acc_row(a, acc, th);
#pragma unroll
      for (int e = 0; e < 8; e++) x[e] += a[e];
      xr[th] = pack8(x);
    }
    ln_part1(xr, sm, L_STAT_B, c);
    u32x4 wa[16];
#pragma unroll
    for (int q = 0; q < 15; q++) wa[q] = lds_r128(sm, L_RING + c.w * 15360 + q * 1024 + c.lane * 16);
    wa[15] = wa15;
    BAR(); ln_part2(xr, sm, L_ACT0, L_STAT_B, c); BAR();
    TS(26);
    zero_acc(acc);
    gemm_tiled(acc, wa, sm, L_ACT0, c);
#pragma unroll
    for (int th = 0; th < 2; th++) { acc_row(v[th], acc, th);
#pragma unroll
      for (int e = 0; e < 8; e++) v[th][e] = silu_f(v[th][e]); }
    act_store(sm, L_ACT1, c, pack8(v[0]), pack8(v[1]));
    BAR();
    TS(27);
    zero_acc(acc);
    gemm_tiled(acc, wb, sm, L_ACT1, c);
#pragma unroll
    for (int th = 0; th < 2; th++) { acc_row(v[th], acc, th);
      const int dest = th * 2 + (c.tok >> 3);
      if (dest != c.s) {
        u32x4 o = pack8(v[th]); o = u32x4{san(o[0]), san(o[1]), san(o[2]), san(o[3])};
        st128(o, rws, dest * 16384 + ((c.w * 4 + c.g) * 8 + (c.tok & 7)) * 16, WS_P2 + (c.tg * 16 + c.s) * 4096);
      } else {
        float x[8]; unpack8(x, xr[th]);
        const int o = L_ACT0 + (c.tok & 7) * 1040 + (c.w * 32 + c.g * 4) * 4;
        *(f32x4*)(sm + o) = f32x4{fmaf(v[th][0], 0.5f, x[0]), fmaf(v[th][1], 0.5f, x[1]), fmaf(v[th][2], 0.5f, x[2]), fmaf(v[th][3], 0.5f, x[3])};
        *(f32x4*)(sm + o + 64) = f32x4{fmaf(v[th][4], 0.5f, x[4]), fmaf(v[th][5], 0.5f, x[5]), fmaf(v[th][6], 0.5f, x[6]), fmaf(v[th][7], 0.5f, x[7])};
      }
    }
  }
  TS(28);
  BAR();
  {
    f32x4 x = *(f32x4*)(sm + L_ACT0 + c.w * 1040 + c.lane * 16);
    u32x2 pp[4];
    for (int sp = 0;; sp++) {
#pragma unroll
      for (int s = 0; s < 4; s++) pp[s] = __builtin_amdgcn_raw_buffer_load_b64(rws, s == c.s ? 0x40000000 : (((c.lane >> 3) * 4 + (c.lane & 3)) * 8 + c.w) * 16 + ((c.lane >> 2) & 1) * 8, WS_P2 + (c.tg * 16 + c.s * 4 + s) * 4096, 16);
      unsigned mx = 0;
#pragma unroll
      for (int s = 0; s < 4; s++) mx = max(mx, max(pp[s][0], pp[s][1]));
      if (dead || !__any(mx == ~0u)) break;
      if (sp > SPIN_CAP) { dead = true; break; }
      asm volatile("" ::: "memory");
    }
    TS(29);
    f32x4 a = {0.f, 0.f, 0.f, 0.f};
#pragma unroll
    for (int s = 0; s < 4; s++) a += f32x4{lo16(pp[s][0]), hi16(pp[s][0]), lo16(pp[s][1]), hi16(pp[s][1])};
    x += 0.5f * a;
    float mean = wave_sum(x[0] + x[1] + x[2] + x[3]) * (1.f / 256.f);
    x -= mean;
    float var = wave_sum(x[0] * x[0] + x[1] * x[1] + x[2] * x[2] + x[3] * x[3]) * (1.f / 256.f);
    float rstd = __builtin_amdgcn_rsqf(var + 1e-5f);
    const int t = c.tile * 32 + c.s * 8 + c.w;
    if (t < T_LEN) *(u32x2*)((char*)p.out + (size_t)(c.b * T_LEN + t) * 512 + c.lane * 8) = u32x2{pack2(x[0] * rstd, x[1] * rstd), pack2(x[2] * rstd, x[3] * rstd)};
  }
  TS(30); VMW(); TS(31); TS_OUT(0);
}

extern "C" void kernel_launch(void* const* d_in, const int* in_sizes, int n_in, void* d_out, int out_size, void* d_ws, size_t ws_size, hipStream_t stream) {
  Params p;
  p.x = (const u16*)d_in[0]; p.w1a = (const u16*)d_in[1]; p.w1b = (const u16*)d_in[2]; p.wqkv = (const u16*)d_in[3]; p.wo = (const u16*)d_in[4];
  p.wp1 = (const u16*)d_in[5]; p.dw = (const u16*)d_in[6]; p.wp2 = (const u16*)d_in[7]; p.w2a = (const u16*)d_in[8]; p.w2b = (const u16*)d_in[9];
  p.out = (u16*)d_out; p.ws = (char*)d_ws;
  k_all<<<256, 512, 0, stream>>>(p);
}
```

```cpp
#include <hip/hip_runtime.h>
#include <hip/hip_bf16.h>

typedef unsigned short u16;
typedef unsigned long long u64;
typedef __attribute__((ext_vector_type(2))) unsigned u32x2;
typedef __attribute__((ext_vector_type(4))) unsigned u32x4;
typedef __attribute__((ext_vector_type(2))) float f32x2;
typedef __attribute__((ext_vector_type(4))) float f32x4;
typedef __attribute__((ext_vector_type(16))) float f32x16;
typedef __attribute__((ext_vector_type(2))) __bf16 bf16x2;
typedef __attribute__((ext_vector_type(4))) __bf16 bf16x4;
typedef __attribute__((ext_vector_type(8))) __bf16 bf16x8;
typedef __attribute__((ext_vector_type(4))) short s16x4;
typedef __amdgpu_buffer_rsrc_t rsrc_t;
#define DI __device__ __forceinline__
#define LAS __attribute__((address_space(3)))
#define SB() __builtin_amdgcn_sched_barrier(0)
#define PIN(x) asm volatile("" : "+v"(x))

constexpr int T_LEN = 1000;
constexpr float LOG2E = 1.4426950408889634f;

constexpr unsigned WS_WT  = 0;
constexpr unsigned WS_SYNC = 1536u << 10;
constexpr unsigned WS_KV  = 2u << 20;
constexpr unsigned WS_G   = 4u << 20;
constexpr unsigned WS_ATT = 5u << 20;
constexpr unsigned WS_P1  = 6u << 20;
constexpr unsigned WS_P2  = WS_P1 + 65536;
constexpr unsigned WT_P2 = 0, WT_2A = 128u << 10, WT_2B = 640u << 10, WT_P1 = 1152u << 10, WT_DW = 1408u << 10;
constexpr unsigned SY_E = 0, SY_F1 = 1024, SY_F2 = SY_F1 + 131072;
constexpr unsigned WS_END = 14u << 20;
constexpr unsigned WS_PROF = 64u << 20;

constexpr int L_RING = 0, L_ACT0 = 131072, L_ACT1 = 147456, L_END = 163840;
constexpr int L_STAT_A = L_ACT1, L_Q = L_ACT1 + 4096, L_COMB = L_ACT0, L_ML = 122880, L_STAT_B = 124928, L_G = L_ACT0;
#define BAR() asm volatile("s_waitcnt lgkmcnt(0)\n\ts_barrier" ::: "memory")
#define VMCNT(n) asm volatile("s_waitcnt vmcnt(" #n ")" ::: "memory")
#define ROT 1

DI rsrc_t mk_rsrc(const void* p, unsigned bytes) { return __builtin_amdgcn_make_buffer_rsrc((void*)p, 0, bytes, 0x00020000); }
DI unsigned pack2(float a, float b) { bf16x2 v = {(__bf16)a, (__bf16)b}; return __builtin_bit_cast(unsigned, v); }
DI float lo16(unsigned u) { return __uint_as_float(u << 16); }
DI float hi16(unsigned u) { return __uint_as_float(u & 0xffff0000u); }
DI f32x4 mfma16(u32x4 a, u32x4 b, f32x4 c) { return __builtin_amdgcn_mfma_f32_16x16x32_bf16(__builtin_bit_cast(bf16x8, a), __builtin_bit_cast(bf16x8, b), c, 0, 0, 0); }
DI f32x16 mfma32(u32x4 a, u32x4 b, f32x16 c) { return __builtin_amdgcn_mfma_f32_32x32x16_bf16(__builtin_bit_cast(bf16x8, a), __builtin_bit_cast(bf16x8, b), c, 0, 0, 0); }
DI f32x4 mfma4(u32x2 a, u32x2 b, f32x4 c) { return __builtin_amdgcn_mfma_f32_4x4x4bf16_1k(__builtin_bit_cast(s16x4, a), __builtin_bit_cast(s16x4, b), c, 0, 0, 0); }
DI float silu_f(float x) { return x * __builtin_amdgcn_rcpf(1.f + __builtin_amdgcn_exp2f(-LOG2E * x)); }
DI float sigm_f(float x) { return __builtin_amdgcn_rcpf(1.f + __builtin_amdgcn_exp2f(-LOG2E * x)); }
DI u32x4 lds_r128(char* sm, int off) { return *(u32x4*)(sm + off); }
DI void lds_w128(char* sm, int off, u32x4 v) { *(u32x4*)(sm + off) = v; }
DI float swadd32(float a, float b) { auto r = __builtin_amdgcn_permlane32_swap(__float_as_uint(a), __float_as_uint(b), false, false); return __uint_as_float(r[0]) + __uint_as_float(r[1]); }
DI float swadd16(float a, float b) { auto r = __builtin_amdgcn_permlane16_swap(__float_as_uint(a), __float_as_uint(b), false, false); return __uint_as_float(r[0]) + __uint_as_float(r[1]); }

struct Ctx { int tid, w, lane, tok, g, b, tile, s, tg; };
DI Ctx mk_ctx() {
  Ctx c; c.tid = threadIdx.x; c.w = __builtin_amdgcn_readfirstlane(threadIdx.x >> 6); c.lane = c.tid & 63; c.tok = c.lane & 15; c.g = c.lane >> 4;
  int bid = blockIdx.x; c.s = (bid >> 3) & 3; c.tg = (bid & 7) * 8 + (bid >> 5); c.b = c.tg >> 5; c.tile = c.tg & 31; return c;
}

DI u32x4 tr_frag(const unsigned* r8, int i) {
  const unsigned sel = i ? 0x07060302u : 0x05040100u;
  u32x4 f;
#pragma unroll
  for (int m = 0; m < 4; m++) f[m] = __builtin_amdgcn_perm(r8[2 * m + 1], r8[2 * m], sel);
  return f;
}
DI void load_tiled(u32x4 (&fr)[16], rsrc_t rs, int voff, int soff) {
#pragma unroll
  for (int q = 0; q < 16; q++) fr[q] = __builtin_amdgcn_raw_buffer_load_b128(rs, voff + (q & 3) * 1024, soff + (q >> 2) * 4096, 16);
}
DI void st128(u32x4 v, rsrc_t rs, unsigned voff, unsigned soff) { __builtin_amdgcn_raw_buffer_store_b128(v, rs, voff + soff, 0, 16); }
DI void dma16(rsrc_t rs, int voff, int soff, int lds) {
  asm volatile("s_mov_b32 m0, %0\n\ts_nop 0\n\tbuffer_load_dwordx4 %1, %2, %3 offen lds" :: "s"(lds), "v"(voff), "s"(rs), "s"(soff) : "memory");
}
DI void dma16s(rsrc_t rs, int voff, int soff, int lds) {
  asm volatile("s_mov_b32 m0, %0\n\ts_nop 0\n\tbuffer_load_dwordx4 %1, %2, %3 offen sc1 lds" :: "s"(lds), "v"(voff), "s"(rs), "s"(soff) : "memory");
}
DI unsigned touch(rsrc_t rs, unsigned voff, bool on = true) { unsigned r; voff |= on ? 0u : 0x40000000u; asm volatile("buffer_load_dword %0, %1, %2, 0 offen sc1" : "=&v"(r) : "v"(voff), "s"(rs) : "memory"); return r; }
DI u32x4 tr_pair(char* sm, int o0, int o1) {
  u32x2 lo = __builtin_bit_cast(u32x2, __builtin_amdgcn_ds_read_tr16_b64_v4bf16((LAS bf16x4*)(sm + o0)));
  u32x2 hi = __builtin_bit_cast(u32x2, __builtin_amdgcn_ds_read_tr16_b64_v4bf16((LAS bf16x4*)(sm + o1)));
  return u32x4{lo[0], lo[1], hi[0], hi[1]};
}
DI void gemm_tiled(f32x4 (&acc)[2][2], const u32x4 (&fr)[16], char* sm, int act, const Ctx& c) {
  const int base = act + (c.g * 32 + c.tok) * 16;
#pragma unroll
  for (int st = 0; st < 8; st++) {
    u32x4 b0 = lds_r128(sm, base + st * 2048), b1 = lds_r128(sm, base + st * 2048 + 256);
#pragma unroll
    for (int i = 0; i < 2; i++) {
      acc[i][0] = mfma16(fr[st * 2 + i], b0, acc[i][0]);
      acc[i][1] = mfma16(fr[st * 2 + i], b1, acc[i][1]);
    }
  }
}
DI void zero_acc(f32x4 (&acc)[2][2]) {
#pragma unroll
  for (int i = 0; i < 2; i++)
#pragma unroll
    for (int t = 0; t < 2; t++) acc[i][t] = f32x4{0.f, 0.f, 0.f, 0.f};
}
DI void unpack8(float (&v)[8], u32x4 p) {
#pragma unroll
  for (int m = 0; m < 4; m++) { v[2 * m] = lo16(p[m]); v[2 * m + 1] = hi16(p[m]); }
}
DI u32x4 pack8(const float (&v)[8]) { return u32x4{pack2(v[0], v[1]), pack2(v[2], v[3]), pack2(v[4], v[5]), pack2(v[6], v[7])}; }
DI void act_store(char* sm, int act, const Ctx& c, u32x4 p0, u32x4 p1) {
  const int o = act + ((c.w * 4 + c.g) * 32 + c.tok) * 16;
  lds_w128(sm, o, p0); lds_w128(sm, o + 256, p1);
}
DI void ln_part1(const u32x4 (&xr)[2], char* sm, int stat, const Ctx& c) {
  float v[2][8]; unpack8(v[0], xr[0]); unpack8(v[1], xr[1]);
  float s0 = 0, s1 = 0, q0 = 0, q1 = 0;
#pragma unroll
  for (int e = 0; e < 8; e++) { s0 += v[0][e]; s1 += v[1][e]; q0 = fmaf(v[0][e], v[0][e], q0); q1 = fmaf(v[1][e], v[1][e], q1); }
  float t = swadd16(swadd32(s0, s1), swadd32(q0, q1));
  *(float*)(sm + stat + (c.w * 64 + c.lane) * 4) = t;
}
DI void ln_part2(const u32x4 (&xr)[2], char* sm, int act, int stat, const Ctx& c) {
  float v[2][8]; unpack8(v[0], xr[0]); unpack8(v[1], xr[1]);
  float u = 0;
#pragma unroll
  for (int k = 0; k < 8; k++) u += *(float*)(sm + stat + (k * 64 + c.lane) * 4);
  auto r = __builtin_amdgcn_permlane16_swap(__float_as_uint(u), __float_as_uint(u), false, false);
  float mean = __uint_as_float(r[0]) * (1.f / 256.f);
  float var = fmaxf(fmaf(-mean, mean, __uint_as_float(r[1]) * (1.f / 256.f)), 0.f);
  float rstd = __builtin_amdgcn_rsqf(var + 1e-5f), nm = -mean * rstd;
  auto rr = __builtin_amdgcn_permlane32_swap(__float_as_uint(rstd), __float_as_uint(rstd), false, false);
  auto rn = __builtin_amdgcn_permlane32_swap(__float_as_uint(nm), __float_as_uint(nm), false, false);
#pragma unroll
  for (int t2 = 0; t2 < 2; t2++)
#pragma unroll
    for (int e = 0; e < 8; e++) v[t2][e] = fmaf(v[t2][e], __uint_as_float(rr[t2]), __uint_as_float(rn[t2]));
  act_store(sm, act, c, pack8(v[0]), pack8(v[1]));
}
DI void acc_row(float (&v)[8], const f32x4 (&acc)[2][2], int th) {
#pragma unroll
  for (int j = 0; j < 4; j++) { v[j] = acc[0][th][j]; v[4 + j] = acc[1][th][j]; }
}
struct Params { const u16 *x, *w1a, *w1b, *wqkv, *wo, *wp1, *dw, *wp2, *w2a, *w2b; u16* out; char* ws; };

DI unsigned ald16(rsrc_t rs, int voff) { unsigned r; asm volatile("buffer_load_ushort %0, %1, %2, 0 offen" : "=&v"(r) : "v"(voff), "s"(rs) : "memory"); return r; }
DI void prep_issue(const Params& p, int task, int lane, int lds, unsigned (&raw)[8]) {
  if (task < 352) {
    const int st = task & 7, r8 = lane >> 3, ck = lane & 7;
    const u16* src; int vo, so, step; unsigned bytes;
    if (task < 32) { src = p.wp2; bytes = 131072; vo = ((r8 >> 2) * 8 + (r8 & 3)) * 512 + ck * 16; so = st * 32 * 512 + (task >> 3) * 128; step = 0; }
    else if (task < 160) { src = p.w2a; bytes = 524288; vo = r8 * 2048 + ck * 16; so = st * 32 * 2048 + ((task - 32) >> 3) * 128; step = 8 * 2048; }
    else if (task < 288) { const int u = (task - 160) >> 3; src = p.w2b; bytes = 524288; vo = r8 * 512 + ck * 16; so = ((u >> 2) * 256 + st * 32) * 512 + (u & 3) * 128; step = 8 * 512; }
    else { src = p.wp1; bytes = 262144; vo = r8 * 1024 + (ck >> 1) * 16 + (ck & 1) * 512; so = st * 32 * 1024 + ((task - 288) >> 3) * 64; step = 8 * 1024; }
    rsrc_t rs = mk_rsrc(src, bytes);
#pragma unroll
    for (int i = 0; i < 4; i++) dma16(rs, vo + so + (task < 32 ? ((i & 1) * 16 + (i >> 1) * 4) * 512 : i * step), 0, lds + i * 1024);
  } else {
    const int t = task - 352, grp = t / 5, m = t - grp * 5;
    const int ch = (16 * grp + (lane >> 2)) & 255, i = lane & 3;
    rsrc_t rs = mk_rsrc(p.dw, 31 * 512);
#pragma unroll
    for (int e = 0; e < 8; e++) raw[e] = ald16(rs, (unsigned)(8 * m + e - 1 - i) < 31u ? (8 * m + e - 1 - i) * 512 + ch * 2 : 0x100000);
  }
}
DI void prep_finish(int task, int lane, char* sm, int lds, const unsigned (&raw)[8], rsrc_t rws) {
  if (task < 352) {
    const int st = task & 7, tb = lds + ((lane >> 4) * 4 + ((lane & 15) >> 2)) * 128 + (lane & 3) * 8;
    const unsigned d = task < 288 ? WS_WT + WT_P2 + (task >> 3) * 32768 + st * 2048 : WS_WT + WT_P1 + ((task - 288) >> 3) * 32768 + st * 1024;
#pragma unroll
    for (int i = 0; i < 4; i++) st128(tr_pair(sm, tb + i * 32, tb + i * 32 + 2048), rws, lane * 16, d + (task < 288 ? (i >> 1) * 16384 + (i & 1) * 1024 : i * 8192));
  } else st128(tr_frag(raw, 0), rws, lane * 16, task < 432 ? WS_WT + WT_DW + (task - 352) * 1024 : 0x40000000u);
}

#define TS_DECL
#define TS(i)
#define TS_OUT(k)
#define VMW()
#define KATTR __global__ __launch_bounds__(512) __attribute__((amdgpu_waves_per_eu(2, 2)))

constexpr int PF_CODE = 27 * 1024;
constexpr int SPIN_CAP = 1 << 17;
#define DRAIN() asm volatile("s_waitcnt vmcnt(0)" ::: "memory")
DI u32x2 ld_flag(rsrc_t rws, int off) { return __builtin_amdgcn_raw_buffer_load_b64(rws, off, WS_SYNC, 16); }
DI void st_flag(rsrc_t rws, int off, u32x2 v) { __builtin_amdgcn_raw_buffer_store_b64(v, rws, off, WS_SYNC, 16); }
DI void poll_flags(rsrc_t rws, int off, u32x2 ep, bool& dead) {
  if (dead) return;
  for (int sp = 0;; sp++) {
    u32x2 f = ld_flag(rws, off);
    if (__all(f[0] == ep[0] && f[1] == ep[1])) break;
    if (sp > SPIN_CAP) { dead = true; break; }
    __builtin_amdgcn_s_sleep(2);
    asm volatile("" ::: "memory");
  }
  asm volatile("" ::: "memory");
}
DI void poll_flags(rsrc_t rws, int off, u32x2 ep, bool& dead, u32x2 first) { if (!__all(first[0] == ep[0] && first[1] == ep[1])) poll_flags(rws, off, ep, dead); asm volatile("" ::: "memory"); }
DI unsigned umax4(u32x4 v) { return max(max(v[0], v[1]), max(v[2], v[3])); }
DI unsigned san(unsigned v) { return min(v, 0xFFFFFFFEu); }

KATTR void k_all(Params p) {
  __shared__ __attribute__((aligned(16))) char sm[L_END];
  TS_DECL; TS(0);
  const Ctx c = mk_ctx();
  rsrc_t rws = mk_rsrc(p.ws, WS_END);
  const unsigned wt = WS_WT;
  bool dead = false;
  u32x2 ep;
  asm volatile("s_nop 4\n\tbuffer_load_dwordx2 %0, %1, %2, 0 offen sc1" : "=&v"(ep) : "v"(WS_SYNC + SY_E), "s"(rws) : "memory");
  u32x4 xr[2];
  f32x4 acc[2][2];
  float v[2][8];
  const int bbase = (c.g * 32 + c.tok) * 16;
    const int rot = ROT ? (c.tg & 3) : 0;
    rsrc_t rx = mk_rsrc(p.x, 2000 * 512), ra = mk_rsrc(p.w1a, 524288), rb = mk_rsrc(p.w1b, 524288), rq = mk_rsrc(p.wqkv, 393216), ro = mk_rsrc(p.wo, 131072);
#pragma unroll
    for (int i = 0; i < 2; i++) {
      const int row = c.w * 4 + i * 2 + (c.lane >> 5), t = c.tile * 32 + row;
      dma16(rx, ((c.b * T_LEN + t) * 512 + (((c.lane & 31) ^ (row & 15)) * 16)) | (t < T_LEN ? 0 : 0x40000000), 0, L_ACT0 + c.w * 2048 + i * 1024);
    }
    int va[4], vb[4];
#pragma unroll
    for (int i = 0; i < 4; i++) { const int row = 2 * i + (c.lane >> 5), ch = (c.lane & 31) ^ (row << 1); va[i] = row * 2048 + ch * 16; vb[i] = row * 512 + ch * 16; }
    const int vq = (c.lane >> 3) * 1536 + (((c.lane & 7) ^ (((c.lane >> 4) & 3) << 1)) * 16);
    auto dma_a = [&](int k) { const int kc = (k + rot) & 3;
#pragma unroll
      for (int i = 0; i < 4; i++) dma16(ra, va[i], (kc * 64 + c.w * 8) * 2048 + c.s * 512, L_RING + k * 32768 + c.w * 4096 + i * 1024); };
    auto dma_b = [&](int k) { const int kc = (k + rot) & 3;
#pragma unroll
      for (int i = 0; i < 4; i++) dma16(rb, vb[i], (c.s * 256 + kc * 64 + c.w * 8) * 512, L_RING + k * 32768 + c.w * 4096 + i * 1024); };
    auto dma_q = [&](int seg) {
#pragma unroll
      for (int i = 0; i < 4; i++) dma16(rq, vq, (c.w * 32 + i * 8) * 1536 + seg * 512 + c.s * 128, L_RING + seg * 32768 + c.w * 4096 + i * 1024); };
    const int trow = c.g * 4 + (c.tok >> 2), tp = c.tok & 3;
    const int tb0 = L_RING + trow * 512 + (((c.w * 4 + (tp >> 1)) ^ ((trow & 7) << 1)) * 16) + (tp & 1) * 8, tb1 = tb0 ^ 32;
    u32x4 fr[16];
    auto tr_chunk = [&](int k) {
#pragma unroll
      for (int h = 0; h < 2; h++) { const int o = k * 32768 + h * 16384;
        fr[(2 * k + h) * 2] = tr_pair(sm, tb0 + o, tb0 + o + 8192); fr[(2 * k + h) * 2 + 1] = tr_pair(sm, tb1 + o, tb1 + o + 8192); } };
    u32x4 bb[16];
    auto act_rd = [&](int k, int act) { const int o = act + bbase + ((k + rot) & 3) * 4096;
#pragma unroll
      for (int h = 0; h < 2; h++) { bb[k * 4 + h * 2] = lds_r128(sm, o + h * 2048); bb[k * 4 + h * 2 + 1] = lds_r128(sm, o + h * 2048 + 256); } };
    auto mm = [&](int q) {
#pragma unroll
      for (int i = 0; i < 2; i++) { acc[i][0] = mfma16(fr[q * 2 + i], bb[2 * q], acc[i][0]); acc[i][1] = mfma16(fr[q * 2 + i], bb[2 * q + 1], acc[i][1]); } };
    auto dma_b1 = [&](int k, int i) { dma16(rb, vb[i], (c.s * 256 + ((k + rot) & 3) * 64 + c.w * 8) * 512, L_RING + k * 32768 + c.w * 4096 + i * 1024); };
    auto dma_q1 = [&](int seg, int i) { dma16(rq, vq, (c.w * 32 + i * 8) * 1536 + seg * 512 + c.s * 128, L_RING + seg * 32768 + c.w * 4096 + i * 1024); };
    dma_a(0); dma_a(1); dma_a(2);
    unsigned pt;
    const int jx = blockIdx.x >> 3;
    {
      const int l = c.lane & 7;
      const char* q = c.w == 0 ? (l == 7 ? (const char*)p.out + (c.b * T_LEN + c.tile * 32) * 512 : l == 6 ? (const char*)p.wqkv : l == 5 ? (const char*)p.w1b + c.s * 131072 : p.ws + (l == 0 ? WS_WT : l == 1 ? WS_KV + c.b * 1048576 : l == 2 ? WS_G : l == 3 ? WS_ATT + c.tg * 16384 : WS_P1 + c.tg * 131072))
                                : (const char*)(__builtin_amdgcn_s_getpc() & ~127ull) + min(jx * 7 + c.w - 1, PF_CODE / 128 - 1) * 128;
      asm volatile("global_load_dword %0, %1, off" : "=&v"(pt) : "v"(q) : "memory");
    }
    TS(1);
    asm volatile("s_waitcnt vmcnt(13)" : "+v"(ep) :: "memory");
    BAR();
#pragma unroll
    for (int th = 0; th < 2; th++) { const int o = L_ACT0 + (th * 16 + c.tok) * 512 + (c.g & 1) * 8, ck = c.w * 4 + (c.g >> 1);
      const u32x2 lo = *(u32x2*)(sm + o + ((ck ^ c.tok) * 16)), hi = *(u32x2*)(sm + o + (((ck + 2) ^ c.tok) * 16));
      xr[th] = u32x4{lo[0], lo[1], hi[0], hi[1]}; }
    dma_a(3);
    { u64 e = (((u64)ep[1] << 32) | ep[0]) + 1; ep[0] = __builtin_amdgcn_readfirstlane((unsigned)e); ep[1] = __builtin_amdgcn_readfirstlane((unsigned)(e >> 32)); }
    ln_part1(xr, sm, L_STAT_A, c);
    VMCNT(9); BAR();
    tr_chunk(0); tr_chunk(1);
    ln_part2(xr, sm, L_ACT0, L_STAT_A, c);
    asm volatile("s_waitcnt vmcnt(0)" : "+v"(pt) :: "memory"); BAR();
    TS(2);
    act_rd(0, L_ACT0); act_rd(1, L_ACT0); SB(); tr_chunk(2); tr_chunk(3); act_rd(2, L_ACT0); act_rd(3, L_ACT0);
    zero_acc(acc);
#pragma unroll
    for (int q = 0; q < 4; q++) { dma_b1(0, q); mm(q); SB(); }
    BAR();
#pragma unroll
    for (int q = 0; q < 4; q++) { dma_b1(1, q); mm(4 + q); SB(); }
    u32x4 hp[2];
#pragma unroll
    for (int th = 0; th < 2; th++) { acc_row(v[th], acc, th);
#pragma unroll
      for (int e = 0; e < 4; e++) { dma_b1(2 + th, e); unsigned u = pack2(silu_f(v[th][2 * e]), silu_f(v[th][2 * e + 1])); PIN(u); hp[th][e] = u; } }
    act_store(sm, L_ACT1, c, hp[0], hp[1]);
    {
      const int t = (c.w - 6) * 256 + blockIdx.x, r = (t & 7) * 32 + (c.lane & 31);
      const char* q = c.w < 6 ? (const char*)p.wqkv + ((jx >> 2) * 32 + (c.w & 1) * 16 + c.tok) * 1536 + (c.w >> 1) * 512 + c.s * 128
        : t < 32 ? (const char*)p.wp2 + r * 512 + (t >> 3) * 128 : t < 160 ? (const char*)p.w2a + r * 2048 + ((t - 32) >> 3) * 128 : t < 288 ? (const char*)p.w2b + ((((t - 160) >> 5) * 256 + r) * 512 + (((t - 160) >> 3) & 3) * 128)
        : t < 352 ? (const char*)p.wp1 + r * 1024 + ((t - 288) >> 3) * 64 + (c.lane >> 5) * 512 : (const char*)p.dw + min(c.lane & 31, 30) * 512 + ((t - 352) / 5 & 15) * 32;
      asm volatile("global_load_dword %0, %1, off" : "=&v"(pt) : "v"(q) : "memory");
    }
    VMCNT(9); BAR();
    TS(3);
    tr_chunk(0); act_rd(0, L_ACT1); SB(); tr_chunk(1); act_rd(1, L_ACT1); SB(); act_rd(2, L_ACT1); act_rd(3, L_ACT1);
    zero_acc(acc);
    mm(0); mm(1); mm(2); mm(3); SB();
    VMCNT(1); BAR();
    tr_chunk(2); tr_chunk(3);
#pragma unroll
    for (int q = 0; q < 4; q++) { dma_q1(0, q); mm(4 + q); SB(); }
  u32x4 own[2];
#pragma unroll
  for (int th = 0; th < 2; th++) { acc_row(v[th], acc, th); own[th] = pack8(v[th]);
    st128(own[th], rws, c.tid * 16 + th * 8192, WS_P1 + (c.tg * 8 + c.s) * 16384); }
  TS(4);
  dma_q(1);
  asm volatile("s_waitcnt vmcnt(4)" : "+v"(pt) :: "memory");
  st_flag(rws, c.lane ? 0x40000000 : SY_F1 + ((c.tg * 4 + c.s) * 8 + c.w) * 64, ep);
  BAR();
  dma_q(2);
  unsigned raw[8];
  const int task = (c.w - 6) * 256 + blockIdx.x, plds = L_RING + 3 * 32768 + (c.w & 1) * 4096;
  if (c.w >= 6) prep_issue(p, task, c.lane, plds, raw);
  else pt = touch(ro, (jx * 32 + c.lane) * 128, (c.tid >> 5) == 10);
  TS(5);
  {
    poll_flags(rws, SY_F1 + ((c.tg * 4 + (c.lane & 3)) * 8 + c.w) * 64, ep, dead);
    TS(7);
    u32x4 pp[3][2];
#pragma unroll
    for (int k = 0; k < 3; k++)
#pragma unroll
      for (int th = 0; th < 2; th++) asm volatile("buffer_load_dwordx4 %0, %1, %2, %3 offen sc1" : "=&v"(pp[k][th]) : "v"(c.tid * 16 + th * 8192), "s"(rws), "s"(WS_P1 + (c.tg * 8 + ((c.s + 1 + k) & 3)) * 16384) : "memory");
    BAR();
    u32x4 wq[16];
    {
      const int ws_ = c.w < 4 ? c.w + 2 : (c.w & 1);
      const int trow = c.g * 4 + (c.tok >> 2), tp = c.tok & 3;
      const int tb0 = L_RING + (ws_ >> 1) * 32768 + trow * 128 + ((((ws_ & 1) * 4 + (tp >> 1)) ^ (((trow >> 1) & 3) << 1)) * 16) + (tp & 1) * 8, tb1 = tb0 ^ 32;
#pragma unroll
      for (int st = 0; st < 8; st++) { wq[2 * st] = tr_pair(sm, tb0 + st * 4096, tb0 + st * 4096 + 2048); wq[2 * st + 1] = tr_pair(sm, tb1 + st * 4096, tb1 + st * 4096 + 2048); }
    }
    if (c.w >= 6) {
      asm volatile("" : "+v"(raw[0]), "+v"(raw[1]), "+v"(raw[2]), "+v"(raw[3]), "+v"(raw[4]), "+v"(raw[5]), "+v"(raw[6]), "+v"(raw[7]) :: "memory");
      prep_finish(task, c.lane, sm, plds, raw, rws);
      if (task < 352) VMCNT(4); else VMCNT(1);
    } else asm volatile("s_waitcnt vmcnt(0)" : "+v"(pt) :: "memory");
    asm volatile("" : "+v"(pp[0][0]), "+v"(pp[0][1]), "+v"(pp[1][0]), "+v"(pp[1][1]), "+v"(pp[2][0]), "+v"(pp[2][1]) :: "memory");
    SB(); TS(8);
#pragma unroll
    for (int th = 0; th < 2; th++) {
      float x[8], a[8], t[8]; unpack8(x, xr[th]); unpack8(a, own[th]);
#pragma unroll
      for (int k = 0; k < 3; k++) { unpack8(t, pp[k][th]);
#pragma unroll
        for (int e = 0; e < 8; e++) a[e] += t[e]; }
#pragma unroll
      for (int e = 0; e < 8; e++) x[e] = fmaf(a[e], 0.5f, x[e]);
      xr[th] = pack8(x);
    }
    TS(9);
    ln_part1(xr, sm, L_STAT_A, c);
    {
      const u32x4 ff = {~0u, ~0u, ~0u, ~0u};
      st128(ff, rws, c.lane * 16, c.w < 4 ? WS_ATT + (c.tg * 4 + c.s) * 4096 + c.w * 1024 : WS_G + (c.b * 64 + c.tile * 2 + ((c.w >> 1) & 1)) * 8192 + c.s * 2048 + (c.w & 1) * 1024);
    }
    BAR();
    dma16(ro, ((c.w & 1) ? vb[2] : vb[0]), (28 + (c.w >> 1)) * 4096, L_RING + (28 + (c.w >> 1)) * 4096 + (c.w & 1) * 2048);
    dma16(ro, ((c.w & 1) ? vb[3] : vb[1]), (28 + (c.w >> 1)) * 4096, L_RING + (28 + (c.w >> 1)) * 4096 + (c.w & 1) * 2048 + 1024);
    ln_part2(xr, sm, L_ACT0, L_STAT_A, c); BAR();
    TS(10);
    zero_acc(acc);
    const unsigned kvb = WS_KV + ((c.b * 4 + c.s) * 32 + c.tile) * 8192;
    if (c.w < 6 && (c.w & 2) == 0) {
      gemm_tiled(acc, wq, sm, L_ACT0, c);
#pragma unroll
      for (int th = 0; th < 2; th++) { acc_row(v[th], acc, th);
        const int o = (((c.w & 1) * 4 + c.g) * 32 + th * 16 + c.tok) * 16;
        if (c.w >= 4) lds_w128(sm, L_Q + o, pack8(v[th]));
        else st128(pack8(v[th]), rws, o, kvb); }
    } else if (c.w < 6) {
#pragma unroll
      for (int st = 0; st < 8; st++) {
        u32x4 b0 = lds_r128(sm, L_ACT0 + bbase + st * 2048), b1 = lds_r128(sm, L_ACT0 + bbase + st * 2048 + 256);
#pragma unroll
        for (int i = 0; i < 2; i++) { acc[i][0] = mfma16(b0, wq[st * 2 + i], acc[i][0]); acc[i][1] = mfma16(b1, wq[st * 2 + i], acc[i][1]); }
      }
#pragma unroll
      for (int th = 0; th < 2; th++) {
        auto r0 = __builtin_amdgcn_permlane32_swap(pack2(acc[0][th][0], acc[0][th][1]), pack2(acc[1][th][0], acc[1][th][1]), false, false);
        auto r1 = __builtin_amdgcn_permlane32_swap(pack2(acc[0][th][2], acc[0][th][3]), pack2(acc[1][th][2], acc[1][th][3]), false, false);
        u32x4 o = {r0[0], r1[0], r0[1], r1[1]};
        st128(o, rws, (8 * (c.tok >> 2) + 4 * (c.lane >> 5) + (c.tok & 3) + 32 * (c.g & 1)) * 16 + ((c.w & 1) * 2 + th) * 1024, kvb + 4096);
      }
    } else {
#pragma unroll 1
      for (int n = 0; n < 7; n++) { const int o = (n * 2 + (c.w & 1)) * 4096;
#pragma unroll
        for (int i = 0; i < 4; i++) dma16(ro, vb[i], o, L_RING + o + i * 1024); }
    }
  }
  TS(11);
  if (c.w < 6) DRAIN(); else VMCNT(28);
  BAR();
  if (c.w == 0) st_flag(rws, c.lane ? 0x40000000 : SY_F2 + (c.tg * 4 + c.s) * 128, ep);
  TS(12);
  u32x4 wo[16], w1[8];
  {
    u32x4 qf[4], kf[4][4], vf[4][4];
#pragma unroll
    for (int ks = 0; ks < 4; ks++) qf[ks] = lds_r128(sm, L_Q + c.lane * 16 + ks * 1024);
    wo[14] = tr_pair(sm, tb0 + 7 * 16384, tb0 + 7 * 16384 + 8192); wo[15] = tr_pair(sm, tb1 + 7 * 16384, tb1 + 7 * 16384 + 8192);
    BAR();
    if (c.w < 7) {
#pragma unroll
      for (int n = 0; n < 8; n++) dma16(ro, vb[n & 3], (14 + c.w * 2 + (n >> 2)) * 4096, L_RING + (14 + c.w * 2) * 4096 + n * 1024);
    }
    poll_flags(rws, SY_F2 + ((c.b * 32 + c.w * 4 + (c.lane & 3)) * 4 + c.s) * 128, ep, dead);
    TS(14);
    const unsigned kvb = WS_KV + ((c.b * 4 + c.s) * 32 + c.w * 4) * 8192;
    __builtin_amdgcn_s_setprio(2);
#pragma unroll
    for (int t = 0; t < 4; t++)
#pragma unroll
      for (int ks = 0; ks < 4; ks++) kf[t][ks] = __builtin_amdgcn_raw_buffer_load_b128(rws, c.lane * 16 + ks * 1024, kvb + t * 8192, 16);
    SB();
    __builtin_amdgcn_s_setprio(1);
    TS(13);
    f32x16 S[4];
    auto ldv = [&](int t) { SB();
#pragma unroll
      for (int ks = 0; ks < 4; ks++) vf[t][ks] = __builtin_amdgcn_raw_buffer_load_b128(rws, c.lane * 16 + ks * 1024, kvb + t * 8192 + 4096, 16);
      SB(); };
#pragma unroll
    for (int t = 0; t < 4; t++) {
#pragma unroll
      for (int e = 0; e < 16; e++) S[t][e] = 0.f;
#pragma unroll
      for (int ks = 0; ks < 4; ks++) S[t] = mfma32(kf[t][ks], qf[ks], S[t]);
    }
    ldv(0);
    __builtin_amdgcn_s_setprio(0);
    TS(15);
    if (c.w == 7) {
#pragma unroll
      for (int e = 4; e < 16; e++) S[3][e] = -INFINITY;
    }
    float m = S[0][0];
#pragma unroll
    for (int t = 0; t < 4; t++)
#pragma unroll
      for (int e = 0; e < 16; e++) m = fmaxf(m, S[t][e]);
    { auto r = __builtin_amdgcn_permlane32_swap(__float_as_uint(m), __float_as_uint(m), false, false); m = fmaxf(__uint_as_float(r[0]), __uint_as_float(r[1])); }
    const float CS = 0.125f * LOG2E;
    float mc = -m * CS; PIN(mc);
    f32x2 l2 = {0.f, 0.f};
    f32x16 O[2];
#pragma unroll
    for (int e = 0; e < 16; e++) { O[0][e] = 0.f; O[1][e] = 0.f; }
    u32x4 pf[4][2]; u32x2 fl;
    auto pv = [&](int t) {
#pragma unroll
      for (int s2 = 0; s2 < 2; s2++) { O[0] = mfma32(vf[t][s2], pf[t][s2], O[0]); O[1] = mfma32(vf[t][2 + s2], pf[t][s2], O[1]); } };
#pragma unroll
    for (int t = 0; t < 4; t++) {
      if (t < 3) ldv(t + 1);
      else { SB();
    {
      const u32x4 ff = {~0u, ~0u, ~0u, ~0u};
      const unsigned o = c.w < 6 ? WS_P2 + (c.tg * 32 + c.s) * 4096 + ((c.s + 1 + (c.w >> 1)) & 3) * 16384 + (c.w & 1) * 2048 : 0x40000000u;
      st128(ff, rws, c.lane * 16, o); st128(ff, rws, c.lane * 16 + 1024, o);
    }
        fl = ld_flag(rws, SY_F2 + ((((c.lane >> 4) & 1) * 32 + c.w * 4) * 4 + (c.lane & 15)) * 128);
        SB(); }
      if (t >= 2) pv(t - 2);
#pragma unroll
      for (int e = 0; e < 16; e += 2) {
        f32x2 z = __builtin_elementwise_fma(f32x2{S[t][e], S[t][e + 1]}, f32x2{CS, CS}, f32x2{mc, mc});
        z = f32x2{__builtin_amdgcn_exp2f(z[0]), __builtin_amdgcn_exp2f(z[1])}; l2 += z; pf[t][e >> 3][(e >> 1) & 3] = pack2(z[0], z[1]); }
      PIN(pf[t][0]); PIN(pf[t][1]); PIN(l2);
    }
    SB(); pv(2); pv(3);
    float l = l2[0] + l2[1]; l = swadd32(l, l);
    if (c.lane < 32) *(f32x2*)(sm + L_ML + (c.w * 32 + c.lane) * 8) = f32x2{m, l};
    const float rl = __builtin_amdgcn_rcpf(l);
#pragma unroll
    for (int dt = 0; dt < 2; dt++)
#pragma unroll
      for (int q = 0; q < 4; q++) *(u32x2*)(sm + L_COMB + ((c.w * 8 + dt * 4 + q) * 64 + c.lane) * 8) = u32x2{pack2(O[dt][4 * q] * rl, O[dt][4 * q + 1] * rl), pack2(O[dt][4 * q + 2] * rl, O[dt][4 * q + 3] * rl)};
    poll_flags(rws, SY_F2 + ((((c.lane >> 4) & 1) * 32 + c.w * 4) * 4 + (c.lane & 15)) * 128, ep, dead, fl);
    TS(16);
    BAR();
    if (c.w == 0 && blockIdx.x == 0) st_flag(rws, c.lane ? 0x40000000 : SY_E, ep);
    TS(17);
    f32x2 ml[8]; u32x2 ok[8]; float M = -INFINITY;
#pragma unroll
    for (int k = 0; k < 8; k++) ml[k] = *(f32x2*)(sm + L_ML + (k * 32 + (c.lane & 31)) * 8);
#pragma unroll
    for (int k = 0; k < 8; k++) ok[k] = *(u32x2*)(sm + L_COMB + ((k * 8 + c.w) * 64 + c.lane) * 8);
#pragma unroll
    for (int k = 0; k < 8; k++) M = fmaxf(M, ml[k][0]);
    float L = 0.f; f32x4 o = {0.f, 0.f, 0.f, 0.f};
#pragma unroll
    for (int k = 0; k < 8; k++) {
      const float f = __builtin_amdgcn_exp2f((ml[k][0] - M) * CS) * ml[k][1];
      L += f;
      o += f * f32x4{lo16(ok[k][0]), hi16(ok[k][0]), lo16(ok[k][1]), hi16(ok[k][1])};
    }
    o *= __builtin_amdgcn_rcpf(L);
    __builtin_amdgcn_raw_buffer_store_b64(u32x2{san(pack2(o[0], o[1])), san(pack2(o[2], o[3]))}, rws, (c.w * 32 + (c.lane & 31)) * 16 + (c.lane >> 5) * 8, WS_ATT + (c.tg * 4 + c.s) * 4096, 16);
    SB();
#pragma unroll
    for (int q = 0; q < 8; q++) w1[q] = __builtin_amdgcn_raw_buffer_load_b128(rws, c.lane * 16 + (q & 3) * 1024, wt + WT_P1 + (c.s * 8 + c.w) * 8192 + (q >> 2) * 4096, 16);
    SB();
#pragma unroll
    for (int st = 0; st < 7; st++) { wo[2 * st] = tr_pair(sm, tb0 + st * 16384, tb0 + st * 16384 + 8192); wo[2 * st + 1] = tr_pair(sm, tb1 + st * 16384, tb1 + st * 16384 + 8192); }
    SB();
  }
  BAR();
  TS(18);
  u32x4 dwf[2][5], wp[16], wb[16], wa15;
  auto LA = [&](int q) { dma16s(rws, c.lane * 16, wt + WT_2A + (c.s * 8 + c.w) * 16384 + q * 1024, L_RING + c.w * 15360 + q * 1024); };
  auto LD = [&](int q) { SB(); dwf[q / 5][q % 5] = __builtin_amdgcn_raw_buffer_load_b128(rws, c.lane * 16 + (q & 3) * 1024, wt + WT_DW + c.w * 10240 + (q >> 2) * 4096, 16); SB(); };
  auto LP = [&](int q) { SB(); wp[q] = __builtin_amdgcn_raw_buffer_load_b128(rws, c.lane * 16 + (q & 3) * 1024, wt + WT_P2 + c.w * 16384 + (q >> 2) * 4096, 16); SB(); };
  auto LB = [&](int q) { SB(); wb[q] = __builtin_amdgcn_raw_buffer_load_b128(rws, c.lane * 16 + (q & 3) * 1024, wt + WT_2B + (c.s * 8 + c.w) * 16384 + (q >> 2) * 4096, 16); SB(); };
  {
    LA(0); LA(1); LA(2);
    u32x4 at[2];
    for (int sp = 0;; sp++) {
#pragma unroll
      for (int h = 0; h < 2; h++) at[h] = __builtin_amdgcn_raw_buffer_load_b128(rws, c.tid * 16 + h * 8192, WS_ATT + c.tg * 16384, 16);
      if (dead || !__any(max(umax4(at[0]), umax4(at[1])) == ~0u)) break;
      if (sp > SPIN_CAP) { dead = true; break; }
      __builtin_amdgcn_s_sleep(3);
      asm volatile("" ::: "memory");
    }
    lds_w128(sm, L_ACT1 + c.tid * 16, at[0]); lds_w128(sm, L_ACT1 + c.tid * 16 + 8192, at[1]);
    LA(3); LA(4);
    TS(19);
    BAR();
    TS(20);
    zero_acc(acc);
    {
      u32x4 bq[16];
#pragma unroll
      for (int st = 0; st < 8; st++) { bq[2 * st] = lds_r128(sm, L_ACT1 + bbase + st * 2048); bq[2 * st + 1] = lds_r128(sm, L_ACT1 + bbase + st * 2048 + 256); }
#pragma unroll
      for (int st = 0; st < 8; st++) {
        if (!(st & 1)) LA(5 + st / 2);
#pragma unroll
        for (int i = 0; i < 2; i++) { acc[i][0] = mfma16(wo[st * 2 + i], bq[2 * st], acc[i][0]); acc[i][1] = mfma16(wo[st * 2 + i], bq[2 * st + 1], acc[i][1]); }
        SB();
      }
    }
#pragma unroll
    for (int th = 0; th < 2; th++) {
      float x[8], a[8]; unpack8(x, xr[th]); acc_row(a, acc, th);
#pragma unroll
      for (int e = 0; e < 8; e++) x[e] += a[e];
      xr[th] = pack8(x);
    }
    PIN(xr[0]); PIN(xr[1]); LA(9);
    ln_part1(xr, sm, L_STAT_B, c);
    LA(10);
    BAR(); LA(11); ln_part2(xr, sm, L_ACT0, L_STAT_B, c); LA(12);
    BAR();
    TS(21);
    f32x4 a2[2] = {f32x4{0.f, 0.f, 0.f, 0.f}, f32x4{0.f, 0.f, 0.f, 0.f}};
    const int base = L_ACT0 + (c.g * 32 + c.tok) * 16;
    u32x4 aq[16];
#pragma unroll
    for (int st = 0; st < 8; st++) { aq[2 * st] = lds_r128(sm, base + st * 2048); aq[2 * st + 1] = lds_r128(sm, base + st * 2048 + 256); }
    LA(13); LA(14);
#pragma unroll
    for (int st = 0; st < 8; st++) { a2[0] = mfma16(aq[2 * st], w1[st], a2[0]); a2[1] = mfma16(aq[2 * st + 1], w1[st], a2[1]); if (st == 3) LD(0); }
    LD(1);
    const bool isA = c.tok < 8;
    const int th = c.tok >> 3;
    float gv[4];
#pragma unroll
    for (int j = 0; j < 4; j++) {
      float snd = isA ? a2[1][j] : a2[0][j];
      float rcv = __uint_as_float(__builtin_amdgcn_update_dpp(0, __float_as_uint(snd), 0x128, 0xf, 0xf, false));
      float a = isA ? a2[0][j] : rcv, b = isA ? rcv : a2[1][j];
      gv[j] = (c.tile * 32 + th * 16 + c.g * 4 + j < T_LEN) ? a * sigm_f(b) : 0.f;
    }
    __builtin_amdgcn_raw_buffer_store_b64(u32x2{san(pack2(gv[0], gv[1])), san(pack2(gv[2], gv[3]))}, rws, th * 8192 + (c.s * 64 + c.w * 8 + (c.tok & 7)) * 32 + c.g * 8, WS_G + (c.b * 64 + c.tile * 2) * 8192, 16);
  }
  BAR();
  TS(22);
  {
#pragma unroll
    for (int q = 2; q < 10; q++) LD(q);
    u32x4 gi[4];
    for (int sp = 0;; sp++) {
#pragma unroll
      for (int q = 0; q < 4; q++) {
        int ht = c.tile * 2 - 1 + q;
        gi[q] = __builtin_amdgcn_raw_buffer_load_b128(rws, (unsigned)ht < 64u ? WS_G + (c.b * 64 + ht) * 8192 + c.tid * 16 : 0x40000000, 0, 16);
      }
      if (dead || !__any(max(max(umax4(gi[0]), umax4(gi[1])), max(umax4(gi[2]), umax4(gi[3]))) == ~0u)) break;
      if (sp > SPIN_CAP) { dead = true; break; }
      __builtin_amdgcn_s_sleep(3);
      asm volatile("" ::: "memory");
    }
#pragma unroll
    for (int q = 0; q < 4; q++) lds_w128(sm, L_G + (c.tid >> 1) * 128 + (((q * 2 + (c.tid & 1)) ^ (c.tid & 4)) * 16), gi[q]);
    LP(0); LP(1); LP(2); LP(3);
    TS(23);
    BAR();
    TS(24);
#pragma unroll
    for (int gq = 0; gq < 2; gq++) {
      const int slot = (c.w * 2 + gq) * 16 + (c.lane >> 2), n = c.lane & 3;
      u32x4 in[5];
#pragma unroll
      for (int m = 0; m < 5; m++) in[m] = lds_r128(sm, L_G + slot * 128 + (((n + m) ^ ((slot & 2) << 1)) * 16));
      LP(4 + gq * 6); LP(5 + gq * 6);
      f32x4 d0 = {0.f, 0.f, 0.f, 0.f}, d1 = {0.f, 0.f, 0.f, 0.f};
#pragma unroll
      for (int j = 0; j < 9; j++) {
        u32x2 a = {dwf[gq][j >> 1][(j & 1) * 2], dwf[gq][j >> 1][(j & 1) * 2 + 1]};
        u32x2 b0 = {in[j >> 1][(j & 1) * 2], in[j >> 1][(j & 1) * 2 + 1]};
        u32x2 b1 = {in[(j + 1) >> 1][((j + 1) & 1) * 2], in[(j + 1) >> 1][((j + 1) & 1) * 2 + 1]};
        d0 = mfma4(a, b0, d0); d1 = mfma4(a, b1, d1);
      }
      LP(6 + gq * 6); LP(7 + gq * 6);
      u32x2 o0 = {pack2(silu_f(d0[0]), silu_f(d0[1])), pack2(silu_f(d0[2]), silu_f(d0[3]))}; PIN(o0);
      LP(8 + gq * 6);
      u32x2 o1 = {pack2(silu_f(d1[0]), silu_f(d1[1])), pack2(silu_f(d1[2]), silu_f(d1[3]))}; PIN(o1);
      const u32x4 o = {o0[0], o0[1], o1[0], o1[1]};
      LP(9 + gq * 6);
      lds_w128(sm, L_G + slot * 128 + (slot & 2) * 32 + (n ^ (((slot >> 3) & 1) * 2)) * 16, o);
    }
    BAR();
    TS(25);
    zero_acc(acc);
    {
      const int q = c.tok >> 2, pp = c.tok & 3;
      const int base = L_G + (c.g * 8 + q) * 128 + (q & 2) * 32 + pp * 8;
      u32x4 bq[16];
#pragma unroll
      for (int st = 0; st < 8; st++)
#pragma unroll
        for (int th = 0; th < 2; th++) { const int o = base + st * 4096 + ((th ^ (c.g & 1)) * 32); bq[2 * st + th] = tr_pair(sm, o, o + 512); }
      SB();
      wa15 = __builtin_amdgcn_raw_buffer_load_b128(rws, c.lane * 16, wt + WT_2A + (c.s * 8 + c.w) * 16384 + 15360, 16);
#pragma unroll
      for (int st = 0; st < 8; st++) {
        if (st & 1) LB(st / 2);
#pragma unroll
        for (int th = 0; th < 2; th++) { acc[0][th] = mfma16(wp[st * 2], bq[2 * st + th], acc[0][th]); acc[1][th] = mfma16(wp[st * 2 + 1], bq[2 * st + th], acc[1][th]); }
        SB();
      }
    }
#pragma unroll
    for (int th = 0; th < 2; th++) {
      float x[8], a[8]; unpack8(x, xr[th]); acc_row(a, acc, th);
#pragma unroll
      for (int e = 0; e < 8; e++) x[e] += a[e];
      xr[th] = pack8(x);
    }
    PIN(xr[0]); PIN(xr[1]); LB(4);
    ln_part1(xr, sm, L_STAT_B, c);
    LB(5);
    u32x4 wa[16];
#pragma unroll
    for (int q = 0; q < 15; q++) wa[q] = lds_r128(sm, L_RING + c.w * 15360 + q * 1024 + c.lane * 16);
    wa[15] = wa15;
    BAR(); LB(6); LB(7); ln_part2(xr, sm, L_ACT0, L_STAT_B, c); LB(8); LB(9); BAR();
    TS(26);
    zero_acc(acc);
    {
      u32x4 bq[16];
#pragma unroll
      for (int st = 0; st < 8; st++) { bq[2 * st] = lds_r128(sm, L_ACT0 + bbase + st * 2048); bq[2 * st + 1] = lds_r128(sm, L_ACT0 + bbase + st * 2048 + 256); }
      LB(10); LB(11);
#pragma unroll
      for (int st = 0; st < 8; st++) {
#pragma unroll
        for (int i = 0; i < 2; i++) { acc[i][0] = mfma16(wa[st * 2 + i], bq[2 * st], acc[i][0]); acc[i][1] = mfma16(wa[st * 2 + i], bq[2 * st + 1], acc[i][1]); }
        if (st & 1) LB(12 + st / 2);
      }
    }
#pragma unroll
    for (int th = 0; th < 2; th++) { acc_row(v[th], acc, th);
#pragma unroll
      for (int e = 0; e < 8; e++) v[th][e] = silu_f(v[th][e]); }
    act_store(sm, L_ACT1, c, pack8(v[0]), pack8(v[1]));
    BAR();
    TS(27);
    zero_acc(acc);
    gemm_tiled(acc, wb, sm, L_ACT1, c);
#pragma unroll
    for (int th = 0; th < 2; th++) { acc_row(v[th], acc, th);
      const int dest = th * 2 + (c.tok >> 3);
      if (dest != c.s) {
        u32x4 o = pack8(v[th]); o = u32x4{san(o[0]), san(o[1]), san(o[2]), san(o[3])};
        st128(o, rws, dest * 16384 + ((c.w * 4 + c.g) * 8 + (c.tok & 7)) * 16, WS_P2 + (c.tg * 32 + c.s) * 4096);
      } else {
        float x[8]; unpack8(x, xr[th]);
        const int o = L_ACT0 + (c.tok & 7) * 1040 + (c.w * 32 + c.g * 4) * 4;
        *(f32x4*)(sm + o) = f32x4{fmaf(v[th][0], 0.5f, x[0]), fmaf(v[th][1], 0.5f, x[1]), fmaf(v[th][2], 0.5f, x[2]), fmaf(v[th][3], 0.5f, x[3])};
        *(f32x4*)(sm + o + 64) = f32x4{fmaf(v[th][4], 0.5f, x[4]), fmaf(v[th][5], 0.5f, x[5]), fmaf(v[th][6], 0.5f, x[6]), fmaf(v[th][7], 0.5f, x[7])};
      }
    }
  }
  TS(28);
  {
    const int tk = (c.lane >> 1) & 7, f0 = c.w * 32 + (c.lane & 1) * 16 + c.g * 4;
    asm volatile("s_waitcnt lgkmcnt(0)" ::: "memory");
    f32x4 x = *(f32x4*)(sm + L_ACT0 + tk * 1040 + f0 * 4);
    u32x2 pp[3];
    for (int sp = 0;; sp++) {
#pragma unroll
      for (int k = 0; k < 3; k++) pp[k] = __builtin_amdgcn_raw_buffer_load_b64(rws, c.tid * 8, WS_P2 + (c.tg * 32 + c.s * 4 + ((c.s + 1 + k) & 3)) * 4096, 16);
      unsigned mx = 0;
#pragma unroll
      for (int k = 0; k < 3; k++) mx = max(mx, max(pp[k][0], pp[k][1]));
      if (dead || !__any(mx == ~0u)) break;
      if (sp > SPIN_CAP) { dead = true; break; }
      __builtin_amdgcn_s_sleep(3);
      asm volatile("" ::: "memory");
    }
    TS(29);
    f32x4 a = {0.f, 0.f, 0.f, 0.f};
#pragma unroll
    for (int k = 0; k < 3; k++) a += f32x4{lo16(pp[k][0]), hi16(pp[k][0]), lo16(pp[k][1]), hi16(pp[k][1])};
    x += 0.5f * a;
    float t = swadd32(x[0] + x[1] + x[2] + x[3], x[0] * x[0] + x[1] * x[1] + x[2] * x[2] + x[3] * x[3]);
    t = swadd16(t, t);
    t += __uint_as_float(__builtin_amdgcn_update_dpp(0, __float_as_uint(t), 0xB1, 0xf, 0xf, false));
    *(float*)(sm + L_STAT_B + c.tid * 4) = t;
    BAR();
    float u = 0.f;
#pragma unroll
    for (int k = 0; k < 8; k++) u += *(float*)(sm + L_STAT_B + (k * 64 + c.lane) * 4);
    auto r = __builtin_amdgcn_permlane32_swap(__float_as_uint(u), __float_as_uint(u), false, false);
    const float mean = __uint_as_float(r[0]) * (1.f / 256.f);
    const float rstd = __builtin_amdgcn_rsqf(fmaxf(fmaf(-mean, mean, __uint_as_float(r[1]) * (1.f / 256.f)), 0.f) + 1e-5f);
    x = (x - mean) * rstd;
    const int tt = c.tile * 32 + c.s * 8 + tk;
    if (tt < T_LEN) *(u32x2*)((char*)p.out + (size_t)(c.b * T_LEN + tt) * 512 + f0 * 2) = u32x2{pack2(x[0], x[1]), pack2(x[2], x[3])};
  }
  TS(30); VMW(); TS(31); TS_OUT(0);
}

extern "C" void kernel_launch(void* const* d_in, const int* in_sizes, int n_in, void* d_out, int out_size, void* d_ws, size_t ws_size, hipStream_t stream) {
  Params p;
  p.x = (const u16*)d_in[0]; p.w1a = (const u16*)d_in[1]; p.w1b = (const u16*)d_in[2]; p.wqkv = (const u16*)d_in[3]; p.wo = (const u16*)d_in[4];
  p.wp1 = (const u16*)d_in[5]; p.dw = (const u16*)d_in[6]; p.wp2 = (const u16*)d_in[7]; p.w2a = (const u16*)d_in[8]; p.w2b = (const u16*)d_in[9];
  p.out = (u16*)d_out; p.ws = (char*)d_ws;
  k_all<<<256, 512, 0, stream>>>(p);
}
```

```cpp
#include <hip/hip_runtime.h>
#include <hip/hip_bf16.h>

typedef unsigned short u16;
typedef unsigned long long u64;
typedef __attribute__((ext_vector_type(2))) unsigned u32x2;
typedef __attribute__((ext_vector_type(4))) unsigned u32x4;
typedef __attribute__((ext_vector_type(2))) float f32x2;
typedef __attribute__((ext_vector_type(4))) float f32x4;
typedef __attribute__((ext_vector_type(16))) float f32x16;
typedef __attribute__((ext_vector_type(2))) __bf16 bf16x2;
typedef __attribute__((ext_vector_type(4))) __bf16 bf16x4;
typedef __attribute__((ext_vector_type(8))) __bf16 bf16x8;
typedef __attribute__((ext_vector_type(4))) short s16x4;
typedef __amdgpu_buffer_rsrc_t rsrc_t;
#define DI __device__ __forceinline__
#define LAS __attribute__((address_space(3)))
#define SB() __builtin_amdgcn_sched_barrier(0)
#define PIN(x) asm volatile("" : "+v"(x))

constexpr int T_LEN = 1000;
constexpr float LOG2E = 1.4426950408889634f;

constexpr unsigned WS_WT  = 0;
constexpr unsigned WS_SYNC = 1536u << 10;
constexpr unsigned WS_KV  = 2u << 20;
constexpr unsigned WS_G   = 4u << 20;
constexpr unsigned WS_ATT = 5u << 20;
constexpr unsigned WS_P1  = 6u << 20;
constexpr unsigned WS_P2  = WS_P1 + 65536;
constexpr unsigned WT_P2 = 0, WT_2A = 128u << 10, WT_2B = 640u << 10, WT_P1 = 1152u << 10, WT_DW = 1408u << 10;
constexpr unsigned SY_E = 0, SY_F1 = 1024, SY_F2 = SY_F1 + 131072;
constexpr unsigned WS_END = 14u << 20;
constexpr unsigned WS_PROF = 64u << 20;

constexpr int L_RING = 0, L_ACT0 = 131072, L_ACT1 = 147456, L_END = 163840;
constexpr int L_STAT_A = L_ACT1, L_Q = L_ACT1 + 4096, L_COMB = L_ACT0, L_ML = 122880, L_STAT_B = 124928, L_G = L_ACT0;
#define BAR() asm volatile("s_waitcnt lgkmcnt(0)\n\ts_barrier" ::: "memory")
#define VMCNT(n) asm volatile("s_waitcnt vmcnt(" #n ")" ::: "memory")
#define OUT_AUX 17
#define ROT 1

DI rsrc_t mk_rsrc(const void* p, unsigned bytes) { return __builtin_amdgcn_make_buffer_rsrc((void*)p, 0, bytes, 0x00020000); }
DI unsigned pack2(float a, float b) { bf16x2 v = {(__bf16)a, (__bf16)b}; return __builtin_bit_cast(unsigned, v); }
DI float lo16(unsigned u) { return __uint_as_float(u << 16); }
DI float hi16(unsigned u) { return __uint_as_float(u & 0xffff0000u); }
DI f32x4 mfma16(u32x4 a, u32x4 b, f32x4 c) { return __builtin_amdgcn_mfma_f32_16x16x32_bf16(__builtin_bit_cast(bf16x8, a), __builtin_bit_cast(bf16x8, b), c, 0, 0, 0); }
DI f32x16 mfma32(u32x4 a, u32x4 b, f32x16 c) { return __builtin_amdgcn_mfma_f32_32x32x16_bf16(__builtin_bit_cast(bf16x8, a), __builtin_bit_cast(bf16x8, b), c, 0, 0, 0); }
DI f32x4 mfma4(u32x2 a, u32x2 b, f32x4 c) { return __builtin_amdgcn_mfma_f32_4x4x4bf16_1k(__builtin_bit_cast(s16x4, a), __builtin_bit_cast(s16x4, b), c, 0, 0, 0); }
DI float silu_f(float x) { return x * __builtin_amdgcn_rcpf(1.f + __builtin_amdgcn_exp2f(-LOG2E * x)); }
DI float sigm_f(float x) { return __builtin_amdgcn_rcpf(1.f + __builtin_amdgcn_exp2f(-LOG2E * x)); }
DI u32x4 lds_r128(char* sm, int off) { return *(u32x4*)(sm + off); }
DI void lds_w128(char* sm, int off, u32x4 v) { *(u32x4*)(sm + off) = v; }
DI float swadd32(float a, float b) { auto r = __builtin_amdgcn_permlane32_swap(__float_as_uint(a), __float_as_uint(b), false, false); return __uint_as_float(r[0]) + __uint_as_float(r[1]); }
DI float swadd16(float a, float b) { auto r = __builtin_amdgcn_permlane16_swap(__float_as_uint(a), __float_as_uint(b), false, false); return __uint_as_float(r[0]) + __uint_as_float(r[1]); }

struct Ctx { int tid, w, lane, tok, g, b, tile, s, tg; };
DI Ctx mk_ctx() {
  Ctx c; c.tid = threadIdx.x; c.w = __builtin_amdgcn_readfirstlane(threadIdx.x >> 6); c.lane = c.tid & 63; c.tok = c.lane & 15; c.g = c.lane >> 4;
  int bid = blockIdx.x; c.s = (bid >> 3) & 3; c.tg = (bid & 7) * 8 + (bid >> 5); c.b = c.tg >> 5; c.tile = c.tg & 31; return c;
}

DI u32x4 tr_frag(const unsigned* r8, int i) {
  const unsigned sel = i ? 0x07060302u : 0x05040100u;
  u32x4 f;
#pragma unroll
  for (int m = 0; m < 4; m++) f[m] = __builtin_amdgcn_perm(r8[2 * m + 1], r8[2 * m], sel);
  return f;
}
DI void load_tiled(u32x4 (&fr)[16], rsrc_t rs, int voff, int soff) {
#pragma unroll
  for (int q = 0; q < 16; q++) fr[q] = __builtin_amdgcn_raw_buffer_load_b128(rs, voff + (q & 3) * 1024, soff + (q >> 2) * 4096, 16);
}
DI void st128(u32x4 v, rsrc_t rs, unsigned voff, unsigned soff) { __builtin_amdgcn_raw_buffer_store_b128(v, rs, voff + soff, 0, 16); }
DI void dma16(rsrc_t rs, int voff, int soff, int lds) {
  asm volatile("s_mov_b32 m0, %0\n\ts_nop 0\n\tbuffer_load_dwordx4 %1, %2, %3 offen lds" :: "s"(lds), "v"(voff), "s"(rs), "s"(soff) : "memory");
}
DI void dma16s(rsrc_t rs, int voff, int soff, int lds) {
  asm volatile("s_mov_b32 m0, %0\n\ts_nop 0\n\tbuffer_load_dwordx4 %1, %2, %3 offen sc1 lds" :: "s"(lds), "v"(voff), "s"(rs), "s"(soff) : "memory");
}
DI unsigned touch(rsrc_t rs, unsigned voff, bool on = true) { unsigned r; voff |= on ? 0u : 0x40000000u; asm volatile("buffer_load_dword %0, %1, %2, 0 offen sc1" : "=&v"(r) : "v"(voff), "s"(rs) : "memory"); return r; }
DI u32x4 tr_pair(char* sm, int o0, int o1) {
  u32x2 lo = __builtin_bit_cast(u32x2, __builtin_amdgcn_ds_read_tr16_b64_v4bf16((LAS bf16x4*)(sm + o0)));
  u32x2 hi = __builtin_bit_cast(u32x2, __builtin_amdgcn_ds_read_tr16_b64_v4bf16((LAS bf16x4*)(sm + o1)));
  return u32x4{lo[0], lo[1], hi[0], hi[1]};
}
DI void gemm_tiled(f32x4 (&acc)[2][2], const u32x4 (&fr)[16], char* sm, int act, const Ctx& c) {
  const int base = act + (c.g * 32 + c.tok) * 16;
#pragma unroll
  for (int st = 0; st < 8; st++) {
    u32x4 b0 = lds_r128(sm, base + st * 2048), b1 = lds_r128(sm, base + st * 2048 + 256);
#pragma unroll
    for (int i = 0; i < 2; i++) {
      acc[i][0] = mfma16(fr[st * 2 + i], b0, acc[i][0]);
      acc[i][1] = mfma16(fr[st * 2 + i], b1, acc[i][1]);
    }
  }
}
DI void zero_acc(f32x4 (&acc)[2][2]) {
#pragma unroll
  for (int i = 0; i < 2; i++)
#pragma unroll
    for (int t = 0; t < 2; t++) acc[i][t] = f32x4{0.f, 0.f, 0.f, 0.f};
}
DI void unpack8(float (&v)[8], u32x4 p) {
#pragma unroll
  for (int m = 0; m < 4; m++) { v[2 * m] = lo16(p[m]); v[2 * m + 1] = hi16(p[m]); }
}
DI u32x4 pack8(const float (&v)[8]) { return u32x4{pack2(v[0], v[1]), pack2(v[2], v[3]), pack2(v[4], v[5]), pack2(v[6], v[7])}; }
DI void act_store(char* sm, int act, const Ctx& c, u32x4 p0, u32x4 p1) {
  const int o = act + ((c.w * 4 + c.g) * 32 + c.tok) * 16;
  lds_w128(sm, o, p0); lds_w128(sm, o + 256, p1);
}
DI void ln_part1(const u32x4 (&xr)[2], char* sm, int stat, const Ctx& c) {
  float v[2][8]; unpack8(v[0], xr[0]); unpack8(v[1], xr[1]);
  float s0 = 0, s1 = 0, q0 = 0, q1 = 0;
#pragma unroll
  for (int e = 0; e < 8; e++) { s0 += v[0][e]; s1 += v[1][e]; q0 = fmaf(v[0][e], v[0][e], q0); q1 = fmaf(v[1][e], v[1][e], q1); }
  float t = swadd16(swadd32(s0, s1), swadd32(q0, q1));
  *(float*)(sm + stat + (c.w * 64 + c.lane) * 4) = t;
}
DI void ln_part2(const u32x4 (&xr)[2], char* sm, int act, int stat, const Ctx& c) {
  float v[2][8]; unpack8(v[0], xr[0]); unpack8(v[1], xr[1]);
  float u = 0;
#pragma unroll
  for (int k = 0; k < 8; k++) u += *(float*)(sm + stat + (k * 64 + c.lane) * 4);
  auto r = __builtin_amdgcn_permlane16_swap(__float_as_uint(u), __float_as_uint(u), false, false);
  float mean = __uint_as_float(r[0]) * (1.f / 256.f);
  float var = fmaxf(fmaf(-mean, mean, __uint_as_float(r[1]) * (1.f / 256.f)), 0.f);
  float rstd = __builtin_amdgcn_rsqf(var + 1e-5f), nm = -mean * rstd;
  auto rr = __builtin_amdgcn_permlane32_swap(__float_as_uint(rstd), __float_as_uint(rstd), false, false);
  auto rn = __builtin_amdgcn_permlane32_swap(__float_as_uint(nm), __float_as_uint(nm), false, false);
#pragma unroll
  for (int t2 = 0; t2 < 2; t2++)
#pragma unroll
    for (int e = 0; e < 8; e++) v[t2][e] = fmaf(v[t2][e], __uint_as_float(rr[t2]), __uint_as_float(rn[t2]));
  act_store(sm, act, c, pack8(v[0]), pack8(v[1]));
}
DI void acc_row(float (&v)[8], const f32x4 (&acc)[2][2], int th) {
#pragma unroll
  for (int j = 0; j < 4; j++) { v[j] = acc[0][th][j]; v[4 + j] = acc[1][th][j]; }
}
struct Params { const u16 *x, *w1a, *w1b, *wqkv, *wo, *wp1, *dw, *wp2, *w2a, *w2b; u16* out; char* ws; };

DI unsigned ald16(rsrc_t rs, int voff) { unsigned r; asm volatile("buffer_load_ushort %0, %1, %2, 0 offen" : "=&v"(r) : "v"(voff), "s"(rs) : "memory"); return r; }
DI void prep_issue(const Params& p, int task, int lane, int lds, unsigned (&raw)[8]) {
  if (task < 352) {
    const int st = task & 7, r8 = lane >> 3, ck = lane & 7;
    const u16* src; int vo, so, step; unsigned bytes;
    if (task < 32) { src = p.wp2; bytes = 131072; vo = ((r8 >> 2) * 8 + (r8 & 3)) * 512 + ck * 16; so = st * 32 * 512 + (task >> 3) * 128; step = 0; }
    else if (task < 160) { src = p.w2a; bytes = 524288; vo = r8 * 2048 + ck * 16; so = st * 32 * 2048 + ((task - 32) >> 3) * 128; step = 8 * 2048; }
    else if (task < 288) { const int u = (task - 160) >> 3; src = p.w2b; bytes = 524288; vo = r8 * 512 + ck * 16; so = ((u >> 2) * 256 + st * 32) * 512 + (u & 3) * 128; step = 8 * 512; }
    else { src = p.wp1; bytes = 262144; vo = r8 * 1024 + (ck >> 1) * 16 + (ck & 1) * 512; so = st * 32 * 1024 + ((task - 288) >> 3) * 64; step = 8 * 1024; }
    rsrc_t rs = mk_rsrc(src, bytes);
#pragma unroll
    for (int i = 0; i < 4; i++) dma16(rs, vo + so + (task < 32 ? ((i & 1) * 16 + (i >> 1) * 4) * 512 : i * step), 0, lds + i * 1024);
  } else {
    const int t = task - 352, grp = t / 5, m = t - grp * 5;
    const int ch = (16 * grp + (lane >> 2)) & 255, i = lane & 3;
    rsrc_t rs = mk_rsrc(p.dw, 31 * 512);
#pragma unroll
    for (int e = 0; e < 8; e++) raw[e] = ald16(rs, (unsigned)(8 * m + e - 1 - i) < 31u ? (8 * m + e - 1 - i) * 512 + ch * 2 : 0x100000);
  }
}
DI void prep_finish(int task, int lane, char* sm, int lds, const unsigned (&raw)[8], rsrc_t rws) {
  if (task < 352) {
    const int st = task & 7, tb = lds + ((lane >> 4) * 4 + ((lane & 15) >> 2)) * 128 + (lane & 3) * 8;
    const unsigned d = task < 288 ? WS_WT + WT_P2 + (task >> 3) * 32768 + st * 2048 : WS_WT + WT_P1 + ((task - 288) >> 3) * 32768 + st * 1024;
#pragma unroll
    for (int i = 0; i < 4; i++) st128(tr_pair(sm, tb + i * 32, tb + i * 32 + 2048), rws, lane * 16, d + (task < 288 ? (i >> 1) * 16384 + (i & 1) * 1024 : i * 8192));
  } else st128(tr_frag(raw, 0), rws, lane * 16, task < 432 ? WS_WT + WT_DW + (task - 352) * 1024 : 0x40000000u);
}

#define TS_DECL
#define TS(i)
#define TS_OUT(k)
#define VMW()
#define KATTR __global__ __launch_bounds__(512) __attribute__((amdgpu_waves_per_eu(2, 2)))

constexpr int PF_CODE = 28 * 1024;
constexpr int SPIN_CAP = 1 << 17;
#define DRAIN() asm volatile("s_waitcnt vmcnt(0)" ::: "memory")
DI u32x2 ld_flag(rsrc_t rws, int off) { return __builtin_amdgcn_raw_buffer_load_b64(rws, off, WS_SYNC, 16); }
DI void st_flag(rsrc_t rws, int off, u32x2 v) { __builtin_amdgcn_raw_buffer_store_b64(v, rws, off, WS_SYNC, 16); }
DI void poll_flags(rsrc_t rws, int off, u32x2 ep, bool& dead) {
  if (dead) return;
  for (int sp = 0;; sp++) {
    u32x2 f = ld_flag(rws, off);
    if (__all(f[0] == ep[0] && f[1] == ep[1])) break;
    if (sp > SPIN_CAP) { dead = true; break; }
    __builtin_amdgcn_s_sleep(2);
    asm volatile("" ::: "memory");
  }
  asm volatile("" ::: "memory");
}
DI void poll_flags(rsrc_t rws, int off, u32x2 ep, bool& dead, u32x2 first) { if (!__all(first[0] == ep[0] && first[1] == ep[1])) poll_flags(rws, off, ep, dead); asm volatile("" ::: "memory"); }
DI unsigned umax4(u32x4 v) { return max(max(v[0], v[1]), max(v[2], v[3])); }
DI unsigned san(unsigned v) { return min(v, 0xFFFFFFFEu); }

KATTR void k_all(Params p) {
  __shared__ __attribute__((aligned(16))) char sm[L_END];
  TS_DECL; TS(0);
  unsigned pt, pt0;
  const int jx = blockIdx.x >> 3;
  {
    const char* q = (const char*)(__builtin_amdgcn_s_getpc() & ~127ull) + min(jx * 8 + (int)(threadIdx.x >> 6), PF_CODE / 128 - 1) * 128;
    asm volatile("global_load_dword %0, %1, off" : "=&v"(pt) : "v"(q) : "memory");
  }
  const Ctx c = mk_ctx();
  rsrc_t rws = mk_rsrc(p.ws, WS_END);
  const unsigned wt = WS_WT;
  bool dead = false;
  u32x2 ep;
  u32x4 xr[2];
  f32x4 acc[2][2];
  float v[2][8];
  const int bbase = (c.g * 32 + c.tok) * 16;
    const int rot = ROT ? (c.tg & 3) : 0;
    rsrc_t rx = mk_rsrc(p.x, 2000 * 512), ra = mk_rsrc(p.w1a, 524288), rb = mk_rsrc(p.w1b, 524288), rq = mk_rsrc(p.wqkv, 393216), ro = mk_rsrc(p.wo, 131072);
#pragma unroll
    for (int i = 0; i < 2; i++) {
      const int row = c.w * 4 + i * 2 + (c.lane >> 5), t = c.tile * 32 + row;
      dma16(rx, ((c.b * T_LEN + t) * 512 + (((c.lane & 31) ^ (row & 15)) * 16)) | (t < T_LEN ? 0 : 0x40000000), 0, L_ACT0 + c.w * 2048 + i * 1024);
    }
    int va[4], vb[4];
#pragma unroll
    for (int i = 0; i < 4; i++) { const int row = 2 * i + (c.lane >> 5), ch = (c.lane & 31) ^ (row << 1); va[i] = row * 2048 + ch * 16; vb[i] = row * 512 + ch * 16; }
    const int vq = (c.lane >> 3) * 1536 + (((c.lane & 7) ^ (((c.lane >> 4) & 3) << 1)) * 16);
    auto dma_a = [&](int k) { const int kc = (k + rot) & 3;
#pragma unroll
      for (int i = 0; i < 4; i++) dma16(ra, va[i], (kc * 64 + c.w * 8) * 2048 + c.s * 512, L_RING + k * 32768 + c.w * 4096 + i * 1024); };
    auto dma_b = [&](int k) { const int kc = (k + rot) & 3;
#pragma unroll
      for (int i = 0; i < 4; i++) dma16(rb, vb[i], (c.s * 256 + kc * 64 + c.w * 8) * 512, L_RING + k * 32768 + c.w * 4096 + i * 1024); };
    auto dma_q = [&](int seg) {
#pragma unroll
      for (int i = 0; i < 4; i++) dma16(rq, vq, (c.w * 32 + i * 8) * 1536 + seg * 512 + c.s * 128, L_RING + seg * 32768 + c.w * 4096 + i * 1024); };
    const int trow = c.g * 4 + (c.tok >> 2), tp = c.tok & 3;
    const int tb0 = L_RING + trow * 512 + (((c.w * 4 + (tp >> 1)) ^ ((trow & 7) << 1)) * 16) + (tp & 1) * 8, tb1 = tb0 ^ 32;
    u32x4 fr[16];
    auto tr_chunk = [&](int k) {
#pragma unroll
      for (int h = 0; h < 2; h++) { const int o = k * 32768 + h * 16384;
        fr[(2 * k + h) * 2] = tr_pair(sm, tb0 + o, tb0 + o + 8192); fr[(2 * k + h) * 2 + 1] = tr_pair(sm, tb1 + o, tb1 + o + 8192); } };
    u32x4 bb[16];
    auto act_rd = [&](int k, int act) { const int o = act + bbase + ((k + rot) & 3) * 4096;
#pragma unroll
      for (int h = 0; h < 2; h++) { bb[k * 4 + h * 2] = lds_r128(sm, o + h * 2048); bb[k * 4 + h * 2 + 1] = lds_r128(sm, o + h * 2048 + 256); } };
    auto mm = [&](int q) {
#pragma unroll
      for (int i = 0; i < 2; i++) { acc[i][0] = mfma16(fr[q * 2 + i], bb[2 * q], acc[i][0]); acc[i][1] = mfma16(fr[q * 2 + i], bb[2 * q + 1], acc[i][1]); } };
    auto dma_b1 = [&](int k, int i) { dma16(rb, vb[i], (c.s * 256 + ((k + rot) & 3) * 64 + c.w * 8) * 512, L_RING + k * 32768 + c.w * 4096 + i * 1024); };
    auto dma_q1 = [&](int seg, int i) { dma16(rq, vq, (c.w * 32 + i * 8) * 1536 + seg * 512 + c.s * 128, L_RING + seg * 32768 + c.w * 4096 + i * 1024); };
    dma_a(0); dma_a(1); dma_a(2);
    TS(1);
    asm volatile("s_waitcnt vmcnt(12)" : "+v"(pt) :: "memory");
    BAR();
#pragma unroll
    for (int th = 0; th < 2; th++) { const int o = L_ACT0 + (th * 16 + c.tok) * 512 + (c.g & 1) * 8, ck = c.w * 4 + (c.g >> 1);
      const u32x2 lo = *(u32x2*)(sm + o + ((ck ^ c.tok) * 16)), hi = *(u32x2*)(sm + o + (((ck + 2) ^ c.tok) * 16));
      xr[th] = u32x4{lo[0], lo[1], hi[0], hi[1]}; }
    dma_a(3);
    ln_part1(xr, sm, L_STAT_A, c);
    VMCNT(8); BAR();
    {
      const char* q = c.w == 0 ? (const char*)p.w1b + c.s * 131072 : c.w == 1 ? (const char*)p.w1b + c.s * 131072 + 65536 : c.w == 2 ? (const char*)p.wqkv : c.w == 3 ? (const char*)p.wo
        : c.w == 4 ? (const char*)p.out + (c.b * T_LEN + c.tile * 32) * 512 : p.ws + (c.w == 5 ? WS_P1 + c.tg * 131072 : c.w == 6 ? WS_KV : WS_G);
      asm volatile("global_load_dword %0, %1, off" : "=&v"(pt0) : "v"(q) : "memory");
    }
    asm volatile("s_nop 4\n\tbuffer_load_dwordx2 %0, %1, %2, 0 offen sc1" : "=&v"(ep) : "v"(WS_SYNC + SY_E), "s"(rws) : "memory");
    tr_chunk(0); tr_chunk(1);
    ln_part2(xr, sm, L_ACT0, L_STAT_A, c);
    VMCNT(2); BAR();
    TS(2);
    act_rd(0, L_ACT0); act_rd(1, L_ACT0); SB(); tr_chunk(2); tr_chunk(3); act_rd(2, L_ACT0); act_rd(3, L_ACT0);
    zero_acc(acc);
#pragma unroll
    for (int q = 0; q < 4; q++) { dma_b1(0, q); mm(q); SB(); }
    BAR();
#pragma unroll
    for (int q = 0; q < 4; q++) { dma_b1(1, q); mm(4 + q); SB(); }
    u32x4 hp[2];
#pragma unroll
    for (int th = 0; th < 2; th++) { acc_row(v[th], acc, th);
#pragma unroll
      for (int e = 0; e < 4; e++) { dma_b1(2 + th, e); unsigned u = pack2(silu_f(v[th][2 * e]), silu_f(v[th][2 * e + 1])); PIN(u); hp[th][e] = u; } }
    act_store(sm, L_ACT1, c, hp[0], hp[1]);
    {
      const int t = (c.w - 6) * 256 + blockIdx.x, r = (t & 7) * 32 + (c.lane & 31);
      const char* q = c.w < 6 ? (const char*)p.wqkv + ((jx >> 2) * 32 + (c.w & 1) * 16 + c.tok) * 1536 + (c.w >> 1) * 512 + c.s * 128
        : t < 32 ? (const char*)p.wp2 + r * 512 + (t >> 3) * 128 : t < 160 ? (const char*)p.w2a + r * 2048 + ((t - 32) >> 3) * 128 : t < 288 ? (const char*)p.w2b + ((((t - 160) >> 5) * 256 + r) * 512 + (((t - 160) >> 3) & 3) * 128)
        : t < 352 ? (const char*)p.wp1 + r * 1024 + ((t - 288) >> 3) * 64 + (c.lane >> 5) * 512 : (const char*)p.dw + min(c.lane & 31, 30) * 512 + ((t - 352) / 5 & 15) * 32;
      asm volatile("global_load_dword %0, %1, off" : "=&v"(pt) : "v"(q) : "memory");
    }
    asm volatile("s_waitcnt vmcnt(9)" : "+v"(ep), "+v"(pt0) :: "memory");
    BAR();
    { u64 e = (((u64)ep[1] << 32) | ep[0]) + 1; ep[0] = __builtin_amdgcn_readfirstlane((unsigned)e); ep[1] = __builtin_amdgcn_readfirstlane((unsigned)(e >> 32)); }
    TS(3);
    tr_chunk(0); act_rd(0, L_ACT1); SB(); tr_chunk(1); act_rd(1, L_ACT1); SB(); act_rd(2, L_ACT1); act_rd(3, L_ACT1);
    zero_acc(acc);
    mm(0); mm(1); mm(2); mm(3); SB();
    VMCNT(1); BAR();
    tr_chunk(2); tr_chunk(3);
#pragma unroll
    for (int q = 0; q < 4; q++) { dma_q1(0, q); mm(4 + q); SB(); }
  u32x4 own[2];
#pragma unroll
  for (int th = 0; th < 2; th++) { acc_row(v[th], acc, th); own[th] = pack8(v[th]);
    st128(own[th], rws, c.tid * 16 + th * 8192, WS_P1 + (c.tg * 8 + c.s) * 16384); }
  TS(4);
  dma_q(1);
  asm volatile("s_waitcnt vmcnt(4)" : "+v"(pt) :: "memory");
  st_flag(rws, c.lane ? 0x40000000 : SY_F1 + ((c.tg * 4 + c.s) * 8 + c.w) * 64, ep);
  BAR();
  dma_q(2);
  unsigned raw[8];
  const int task = (c.w - 6) * 256 + blockIdx.x, plds = L_RING + 3 * 32768 + (c.w & 1) * 4096;
  if (c.w >= 6) prep_issue(p, task, c.lane, plds, raw);
  else pt = touch(ro, (jx * 32 + c.lane) * 128, (c.tid >> 5) == 10);
  TS(5);
  {
    poll_flags(rws, SY_F1 + ((c.tg * 4 + (c.lane & 3)) * 8 + c.w) * 64, ep, dead);
    TS(7);
    u32x4 pp[3][2];
#pragma unroll
    for (int k = 0; k < 3; k++)
#pragma unroll
      for (int th = 0; th < 2; th++) asm volatile("buffer_load_dwordx4 %0, %1, %2, %3 offen sc1" : "=&v"(pp[k][th]) : "v"(c.tid * 16 + th * 8192), "s"(rws), "s"(WS_P1 + (c.tg * 8 + ((c.s + 1 + k) & 3)) * 16384) : "memory");
    BAR();
    u32x4 wq[16];
    {
      const int ws_ = c.w < 4 ? c.w + 2 : (c.w & 1);
      const int trow = c.g * 4 + (c.tok >> 2), tp = c.tok & 3;
      const int tb0 = L_RING + (ws_ >> 1) * 32768 + trow * 128 + ((((ws_ & 1) * 4 + (tp >> 1)) ^ (((trow >> 1) & 3) << 1)) * 16) + (tp & 1) * 8, tb1 = tb0 ^ 32;
#pragma unroll
      for (int st = 0; st < 8; st++) { wq[2 * st] = tr_pair(sm, tb0 + st * 4096, tb0 + st * 4096 + 2048); wq[2 * st + 1] = tr_pair(sm, tb1 + st * 4096, tb1 + st * 4096 + 2048); }
    }
    if (c.w >= 6) {
      asm volatile("" : "+v"(raw[0]), "+v"(raw[1]), "+v"(raw[2]), "+v"(raw[3]), "+v"(raw[4]), "+v"(raw[5]), "+v"(raw[6]), "+v"(raw[7]) :: "memory");
      prep_finish(task, c.lane, sm, plds, raw, rws);
      if (task < 352) VMCNT(4); else VMCNT(1);
    } else asm volatile("s_waitcnt vmcnt(0)" : "+v"(pt) :: "memory");
    asm volatile("" : "+v"(pp[0][0]), "+v"(pp[0][1]), "+v"(pp[1][0]), "+v"(pp[1][1]), "+v"(pp[2][0]), "+v"(pp[2][1]) :: "memory");
    SB(); TS(8);
#pragma unroll
    for (int th = 0; th < 2; th++) {
      float x[8], a[8], t[8]; unpack8(x, xr[th]); unpack8(a, own[th]);
#pragma unroll
      for (int k = 0; k < 3; k++) { unpack8(t, pp[k][th]);
#pragma unroll
        for (int e = 0; e < 8; e++) a[e] += t[e]; }
#pragma unroll
      for (int e = 0; e < 8; e++) x[e] = fmaf(a[e], 0.5f, x[e]);
      xr[th] = pack8(x);
    }
    TS(9);
    ln_part1(xr, sm, L_STAT_A, c);
    {
      const u32x4 ff = {~0u, ~0u, ~0u, ~0u};
      st128(ff, rws, c.lane * 16, c.w < 4 ? WS_ATT + (c.tg * 4 + c.s) * 4096 + c.w * 1024 : WS_G + (c.b * 64 + c.tile * 2 + ((c.w >> 1) & 1)) * 8192 + c.s * 2048 + (c.w & 1) * 1024);
    }
    BAR();
    dma16(ro, ((c.w & 1) ? vb[2] : vb[0]), (28 + (c.w >> 1)) * 4096, L_RING + (28 + (c.w >> 1)) * 4096 + (c.w & 1) * 2048);
    dma16(ro, ((c.w & 1) ? vb[3] : vb[1]), (28 + (c.w >> 1)) * 4096, L_RING + (28 + (c.w >> 1)) * 4096 + (c.w & 1) * 2048 + 1024);
    ln_part2(xr, sm, L_ACT0, L_STAT_A, c); BAR();
    TS(10);
    zero_acc(acc);
    const unsigned kvb = WS_KV + ((c.b * 4 + c.s) * 32 + c.tile) * 8192;
    if (c.w < 6 && (c.w & 2) == 0) {
      gemm_tiled(acc, wq, sm, L_ACT0, c);
#pragma unroll
      for (int th = 0; th < 2; th++) { acc_row(v[th], acc, th);
        const int o = (((c.w & 1) * 4 + c.g) * 32 + th * 16 + c.tok) * 16;
        if (c.w >= 4) lds_w128(sm, L_Q + o, pack8(v[th]));
        else st128(pack8(v[th]), rws, o, kvb); }
    } else if (c.w < 6) {
#pragma unroll
      for (int st = 0; st < 8; st++) {
        u32x4 b0 = lds_r128(sm, L_ACT0 + bbase + st * 2048), b1 = lds_r128(sm, L_ACT0 + bbase + st * 2048 + 256);
#pragma unroll
        for (int i = 0; i < 2; i++) { acc[i][0] = mfma16(b0, wq[st * 2 + i], acc[i][0]); acc[i][1] = mfma16(b1, wq[st * 2 + i], acc[i][1]); }
      }
#pragma unroll
      for (int th = 0; th < 2; th++) {
        auto r0 = __builtin_amdgcn_permlane32_swap(pack2(acc[0][th][0], acc[0][th][1]), pack2(acc[1][th][0], acc[1][th][1]), false, false);
        auto r1 = __builtin_amdgcn_permlane32_swap(pack2(acc[0][th][2], acc[0][th][3]), pack2(acc[1][th][2], acc[1][th][3]), false, false);
        u32x4 o = {r0[0], r1[0], r0[1], r1[1]};
        st128(o, rws, (8 * (c.tok >> 2) + 4 * (c.lane >> 5) + (c.tok & 3) + 32 * (c.g & 1)) * 16 + ((c.w & 1) * 2 + th) * 1024, kvb + 4096);
      }
    } else {
#pragma unroll 1
      for (int n = 0; n < 7; n++) { const int o = (n * 2 + (c.w & 1)) * 4096;
#pragma unroll
        for (int i = 0; i < 4; i++) dma16(ro, vb[i], o, L_RING + o + i * 1024); }
    }
  }
  TS(11);
  if (c.w < 6) DRAIN(); else VMCNT(28);
  BAR();
  if (c.w == 0) st_flag(rws, c.lane ? 0x40000000 : SY_F2 + (c.tg * 4 + c.s) * 128, ep);
  TS(12);
  u32x4 wo[16], w1[8];
  {
    u32x4 qf[4], kf[4][4], vf[4][4];
#pragma unroll
    for (int ks = 0; ks < 4; ks++) qf[ks] = lds_r128(sm, L_Q + c.lane * 16 + ks * 1024);
    wo[14] = tr_pair(sm, tb0 + 7 * 16384, tb0 + 7 * 16384 + 8192); wo[15] = tr_pair(sm, tb1 + 7 * 16384, tb1 + 7 * 16384 + 8192);
    BAR();
    if (c.w < 7) {
#pragma unroll
      for (int n = 0; n < 8; n++) dma16(ro, vb[n & 3], (14 + c.w * 2 + (n >> 2)) * 4096, L_RING + (14 + c.w * 2) * 4096 + n * 1024);
    }
    poll_flags(rws, SY_F2 + ((c.b * 32 + c.w * 4 + (c.lane & 3)) * 4 + c.s) * 128, ep, dead);
    TS(14);
    const unsigned kvb = WS_KV + ((c.b * 4 + c.s) * 32 + c.w * 4) * 8192;
    __builtin_amdgcn_s_setprio(2);
#pragma unroll
    for (int t = 0; t < 4; t++)
#pragma unroll
      for (int ks = 0; ks < 4; ks++) kf[t][ks] = __builtin_amdgcn_raw_buffer_load_b128(rws, c.lane * 16 + ks * 1024, kvb + t * 8192, 16);
    SB();
    __builtin_amdgcn_s_setprio(1);
    TS(13);
    f32x16 S[4];
    auto ldv = [&](int t) { SB();
#pragma unroll
      for (int ks = 0; ks < 4; ks++) vf[t][ks] = __builtin_amdgcn_raw_buffer_load_b128(rws, c.lane * 16 + ks * 1024, kvb + t * 8192 + 4096, 16);
      SB(); };
#pragma unroll
    for (int t = 0; t < 4; t++) {
#pragma unroll
      for (int e = 0; e < 16; e++) S[t][e] = 0.f;
#pragma unroll
      for (int ks = 0; ks < 4; ks++) S[t] = mfma32(kf[t][ks], qf[ks], S[t]);
    }
    ldv(0);
    __builtin_amdgcn_s_setprio(0);
    TS(15);
    if (c.w == 7) {
#pragma unroll
      for (int e = 4; e < 16; e++) S[3][e] = -INFINITY;
    }
    float m = S[0][0];
#pragma unroll
    for (int t = 0; t < 4; t++)
#pragma unroll
      for (int e = 0; e < 16; e++) m = fmaxf(m, S[t][e]);
    { auto r = __builtin_amdgcn_permlane32_swap(__float_as_uint(m), __float_as_uint(m), false, false); m = fmaxf(__uint_as_float(r[0]), __uint_as_float(r[1])); }
    const float CS = 0.125f * LOG2E;
    float mc = -m * CS; PIN(mc);
    f32x2 l2 = {0.f, 0.f};
    f32x16 O[2];
#pragma unroll
    for (int e = 0; e < 16; e++) { O[0][e] = 0.f; O[1][e] = 0.f; }
    u32x4 pf[4][2]; u32x2 fl;
    auto pv = [&](int t) {
#pragma unroll
      for (int s2 = 0; s2 < 2; s2++) { O[0] = mfma32(vf[t][s2], pf[t][s2], O[0]); O[1] = mfma32(vf[t][2 + s2], pf[t][s2], O[1]); } };
#pragma unroll
    for (int t = 0; t < 4; t++) {
      if (t < 3) ldv(t + 1);
      else { SB();
    {
      const u32x4 ff = {~0u, ~0u, ~0u, ~0u};
      const unsigned o = c.w < 6 ? WS_P2 + (c.tg * 32 + c.s) * 4096 + ((c.s + 1 + (c.w >> 1)) & 3) * 16384 + (c.w & 1) * 2048 : 0x40000000u;
      st128(ff, rws, c.lane * 16, o); st128(ff, rws, c.lane * 16 + 1024, o);
    }
        fl = ld_flag(rws, SY_F2 + ((((c.lane >> 4) & 1) * 32 + c.w * 4) * 4 + (c.lane & 15)) * 128);
        SB(); }
      if (t >= 2) pv(t - 2);
#pragma unroll
      for (int e = 0; e < 16; e += 2) {
        f32x2 z = __builtin_elementwise_fma(f32x2{S[t][e], S[t][e + 1]}, f32x2{CS, CS}, f32x2{mc, mc});
        z = f32x2{__builtin_amdgcn_exp2f(z[0]), __builtin_amdgcn_exp2f(z[1])}; l2 += z; pf[t][e >> 3][(e >> 1) & 3] = pack2(z[0], z[1]); }
      PIN(pf[t][0]); PIN(pf[t][1]); PIN(l2);
    }
    SB(); pv(2); pv(3);
    float l = l2[0] + l2[1]; l = swadd32(l, l);
    if (c.lane < 32) *(f32x2*)(sm + L_ML + (c.w * 32 + c.lane) * 8) = f32x2{m, l};
    const float rl = __builtin_amdgcn_rcpf(l);
#pragma unroll
    for (int dt = 0; dt < 2; dt++)
#pragma unroll
      for (int q = 0; q < 4; q++) *(u32x2*)(sm + L_COMB + ((c.w * 8 + dt * 4 + q) * 64 + c.lane) * 8) = u32x2{pack2(O[dt][4 * q] * rl, O[dt][4 * q + 1] * rl), pack2(O[dt][4 * q + 2] * rl, O[dt][4 * q + 3] * rl)};
    poll_flags(rws, SY_F2 + ((((c.lane >> 4) & 1) * 32 + c.w * 4) * 4 + (c.lane & 15)) * 128, ep, dead, fl);
    TS(16);
    BAR();
    if (c.w == 0 && blockIdx.x == 0) st_flag(rws, c.lane ? 0x40000000 : SY_E, ep);
    TS(17);
    f32x2 ml[8]; u32x2 ok[8]; float M = -INFINITY;
#pragma unroll
    for (int k = 0; k < 8; k++) ml[k] = *(f32x2*)(sm + L_ML + (k * 32 + (c.lane & 31)) * 8);
#pragma unroll
    for (int k = 0; k < 8; k++) ok[k] = *(u32x2*)(sm + L_COMB + ((k * 8 + c.w) * 64 + c.lane) * 8);
#pragma unroll
    for (int k = 0; k < 8; k++) M = fmaxf(M, ml[k][0]);
    float L = 0.f; f32x4 o = {0.f, 0.f, 0.f, 0.f};
#pragma unroll
    for (int k = 0; k < 8; k++) {
      const float f = __builtin_amdgcn_exp2f((ml[k][0] - M) * CS) * ml[k][1];
      L += f;
      o += f * f32x4{lo16(ok[k][0]), hi16(ok[k][0]), lo16(ok[k][1]), hi16(ok[k][1])};
    }
    o *= __builtin_amdgcn_rcpf(L);
    __builtin_amdgcn_raw_buffer_store_b64(u32x2{san(pack2(o[0], o[1])), san(pack2(o[2], o[3]))}, rws, (c.w * 32 + (c.lane & 31)) * 16 + (c.lane >> 5) * 8, WS_ATT + (c.tg * 4 + c.s) * 4096, 16);
    SB();
#pragma unroll
    for (int q = 0; q < 8; q++) w1[q] = __builtin_amdgcn_raw_buffer_load_b128(rws, c.lane * 16 + (q & 3) * 1024, wt + WT_P1 + (c.s * 8 + c.w) * 8192 + (q >> 2) * 4096, 16);
    SB();
#pragma unroll
    for (int st = 0; st < 7; st++) { wo[2 * st] = tr_pair(sm, tb0 + st * 16384, tb0 + st * 16384 + 8192); wo[2 * st + 1] = tr_pair(sm, tb1 + st * 16384, tb1 + st * 16384 + 8192); }
    SB();
  }
  BAR();
  TS(18);
  u32x4 dwf[2][5], wp[16], wb[16], wa15;
  auto LA = [&](int q) { dma16s(rws, c.lane * 16, wt + WT_2A + (c.s * 8 + c.w) * 16384 + q * 1024, L_RING + c.w * 15360 + q * 1024); };
  auto LD = [&](int q) { SB(); dwf[q / 5][q % 5] = __builtin_amdgcn_raw_buffer_load_b128(rws, c.lane * 16 + (q & 3) * 1024, wt + WT_DW + c.w * 10240 + (q >> 2) * 4096, 16); SB(); };
  auto LP = [&](int q) { SB(); wp[q] = __builtin_amdgcn_raw_buffer_load_b128(rws, c.lane * 16 + (q & 3) * 1024, wt + WT_P2 + c.w * 16384 + (q >> 2) * 4096, 16); SB(); };
  auto LB = [&](int q) { SB(); wb[q] = __builtin_amdgcn_raw_buffer_load_b128(rws, c.lane * 16 + (q & 3) * 1024, wt + WT_2B + (c.s * 8 + c.w) * 16384 + (q >> 2) * 4096, 16); SB(); };
  {
    LA(0); LA(1); LA(2);
    u32x4 at[2];
    for (int sp = 0;; sp++) {
#pragma unroll
      for (int h = 0; h < 2; h++) at[h] = __builtin_amdgcn_raw_buffer_load_b128(rws, c.tid * 16 + h * 8192, WS_ATT + c.tg * 16384, 16);
      if (dead || !__any(max(umax4(at[0]), umax4(at[1])) == ~0u)) break;
      if (sp > SPIN_CAP) { dead = true; break; }
      __builtin_amdgcn_s_sleep(3);
      asm volatile("" ::: "memory");
    }
    lds_w128(sm, L_ACT1 + c.tid * 16, at[0]); lds_w128(sm, L_ACT1 + c.tid * 16 + 8192, at[1]);
    LA(3); LA(4);
    TS(19);
    BAR();
    TS(20);
    zero_acc(acc);
    {
      u32x4 bq[16];
#pragma unroll
      for (int st = 0; st < 8; st++) { bq[2 * st] = lds_r128(sm, L_ACT1 + bbase + st * 2048); bq[2 * st + 1] = lds_r128(sm, L_ACT1 + bbase + st * 2048 + 256); }
#pragma unroll
      for (int st = 0; st < 8; st++) {
        if (!(st & 1)) LA(5 + st / 2);
#pragma unroll
        for (int i = 0; i < 2; i++) { acc[i][0] = mfma16(wo[st * 2 + i], bq[2 * st], acc[i][0]); acc[i][1] = mfma16(wo[st * 2 + i], bq[2 * st + 1], acc[i][1]); }
        SB();
      }
    }
#pragma unroll
    for (int th = 0; th < 2; th++) {
      float x[8], a[8]; unpack8(x, xr[th]); acc_row(a, acc, th);
#pragma unroll
      for (int e = 0; e < 8; e++) x[e] += a[e];
      xr[th] = pack8(x);
    }
    PIN(xr[0]); PIN(xr[1]); LA(9);
    ln_part1(xr, sm, L_STAT_B, c);
    LA(10);
    BAR(); LA(11); ln_part2(xr, sm, L_ACT0, L_STAT_B, c); LA(12);
    BAR();
    TS(21);
    f32x4 a2[2] = {f32x4{0.f, 0.f, 0.f, 0.f}, f32x4{0.f, 0.f, 0.f, 0.f}};
    const int base = L_ACT0 + (c.g * 32 + c.tok) * 16;
    u32x4 aq[16];
#pragma unroll
    for (int st = 0; st < 8; st++) { aq[2 * st] = lds_r128(sm, base + st * 2048); aq[2 * st + 1] = lds_r128(sm, base + st * 2048 + 256); }
    LA(13); LA(14);
#pragma unroll
    for (int st = 0; st < 8; st++) { a2[0] = mfma16(aq[2 * st], w1[st], a2[0]); a2[1] = mfma16(aq[2 * st + 1], w1[st], a2[1]); if (st == 3) LD(0); }
    LD(1);
    const bool isA = c.tok < 8;
    const int th = c.tok >> 3;
    float gv[4];
#pragma unroll
    for (int j = 0; j < 4; j++) {
      float snd = isA ? a2[1][j] : a2[0][j];
      float rcv = __uint_as_float(__builtin_amdgcn_update_dpp(0, __float_as_uint(snd), 0x128, 0xf, 0xf, false));
      float a = isA ? a2[0][j] : rcv, b = isA ? rcv : a2[1][j];
      gv[j] = (c.tile * 32 + th * 16 + c.g * 4 + j < T_LEN) ? a * sigm_f(b) : 0.f;
    }
    __builtin_amdgcn_raw_buffer_store_b64(u32x2{san(pack2(gv[0], gv[1])), san(pack2(gv[2], gv[3]))}, rws, th * 8192 + (c.s * 64 + c.w * 8 + (c.tok & 7)) * 32 + c.g * 8, WS_G + (c.b * 64 + c.tile * 2) * 8192, 16);
  }
  BAR();
  TS(22);
  {
#pragma unroll
    for (int q = 2; q < 10; q++) LD(q);
    u32x4 gi[4];
    for (int sp = 0;; sp++) {
#pragma unroll
      for (int q = 0; q < 4; q++) {
        int ht = c.tile * 2 - 1 + q;
        gi[q] = __builtin_amdgcn_raw_buffer_load_b128(rws, (unsigned)ht < 64u ? WS_G + (c.b * 64 + ht) * 8192 + c.tid * 16 : 0x40000000, 0, 16);
      }
      if (dead || !__any(max(max(umax4(gi[0]), umax4(gi[1])), max(umax4(gi[2]), umax4(gi[3]))) == ~0u)) break;
      if (sp > SPIN_CAP) { dead = true; break; }
      __builtin_amdgcn_s_sleep(3);
      asm volatile("" ::: "memory");
    }
#pragma unroll
    for (int q = 0; q < 4; q++) lds_w128(sm, L_G + (c.tid >> 1) * 128 + (((q * 2 + (c.tid & 1)) ^ (c.tid & 4)) * 16), gi[q]);
    LP(0); LP(1); LP(2); LP(3);
    TS(23);
    BAR();
    TS(24);
#pragma unroll
    for (int gq = 0; gq < 2; gq++) {
      const int slot = (c.w * 2 + gq) * 16 + (c.lane >> 2), n = c.lane & 3;
      u32x4 in[5];
#pragma unroll
      for (int m = 0; m < 5; m++) in[m] = lds_r128(sm, L_G + slot * 128 + (((n + m) ^ ((slot & 2) << 1)) * 16));
      LP(4 + gq * 6); LP(5 + gq * 6);
      f32x4 d0 = {0.f, 0.f, 0.f, 0.f}, d1 = {0.f, 0.f, 0.f, 0.f};
#pragma unroll
      for (int j = 0; j < 9; j++) {
        u32x2 a = {dwf[gq][j >> 1][(j & 1) * 2], dwf[gq][j >> 1][(j & 1) * 2 + 1]};
        u32x2 b0 = {in[j >> 1][(j & 1) * 2], in[j >> 1][(j & 1) * 2 + 1]};
        u32x2 b1 = {in[(j + 1) >> 1][((j + 1) & 1) * 2], in[(j + 1) >> 1][((j + 1) & 1) * 2 + 1]};
        d0 = mfma4(a, b0, d0); d1 = mfma4(a, b1, d1);
      }
      LP(6 + gq * 6); LP(7 + gq * 6);
      u32x2 o0 = {pack2(silu_f(d0[0]), silu_f(d0[1])), pack2(silu_f(d0[2]), silu_f(d0[3]))}; PIN(o0);
      LP(8 + gq * 6);
      u32x2 o1 = {pack2(silu_f(d1[0]), silu_f(d1[1])), pack2(silu_f(d1[2]), silu_f(d1[3]))}; PIN(o1);
      const u32x4 o = {o0[0], o0[1], o1[0], o1[1]};
      LP(9 + gq * 6);
      lds_w128(sm, L_G + slot * 128 + (slot & 2) * 32 + (n ^ (((slot >> 3) & 1) * 2)) * 16, o);
    }
    BAR();
    TS(25);
    zero_acc(acc);
    {
      const int q = c.tok >> 2, pp = c.tok & 3;
      const int base = L_G + (c.g * 8 + q) * 128 + (q & 2) * 32 + pp * 8;
      u32x4 bq[16];
#pragma unroll
      for (int st = 0; st < 8; st++)
#pragma unroll
        for (int th = 0; th < 2; th++) { const int o = base + st * 4096 + ((th ^ (c.g & 1)) * 32); bq[2 * st + th] = tr_pair(sm, o, o + 512); }
      SB();
      wa15 = __builtin_amdgcn_raw_buffer_load_b128(rws, c.lane * 16, wt + WT_2A + (c.s * 8 + c.w) * 16384 + 15360, 16);
#pragma unroll
      for (int st = 0; st < 8; st++) {
        if (st & 1) LB(st / 2);
#pragma unroll
        for (int th = 0; th < 2; th++) { acc[0][th] = mfma16(wp[st * 2], bq[2 * st + th], acc[0][th]); acc[1][th] = mfma16(wp[st * 2 + 1], bq[2 * st + th], acc[1][th]); }
        SB();
      }
    }
#pragma unroll
    for (int th = 0; th < 2; th++) {
      float x[8], a[8]; unpack8(x, xr[th]); acc_row(a, acc, th);
#pragma unroll
      for (int e = 0; e < 8; e++) x[e] += a[e];
      xr[th] = pack8(x);
    }
    PIN(xr[0]); PIN(xr[1]); LB(4);
    ln_part1(xr, sm, L_STAT_B, c);
    LB(5);
    u32x4 wa[16];
#pragma unroll
    for (int q = 0; q < 15; q++) wa[q] = lds_r128(sm, L_RING + c.w * 15360 + q * 1024 + c.lane * 16);
    wa[15] = wa15;
    BAR(); LB(6); LB(7); ln_part2(xr, sm, L_ACT0, L_STAT_B, c); LB(8); LB(9); BAR();
    TS(26);
    zero_acc(acc);
    {
      u32x4 bq[16];
#pragma unroll
      for (int st = 0; st < 8; st++) { bq[2 * st] = lds_r128(sm, L_ACT0 + bbase + st * 2048); bq[2 * st + 1] = lds_r128(sm, L_ACT0 + bbase + st * 2048 + 256); }
      LB(10); LB(11);
#pragma unroll
      for (int st = 0; st < 8; st++) {
#pragma unroll
        for (int i = 0; i < 2; i++) { acc[i][0] = mfma16(wa[st * 2 + i], bq[2 * st], acc[i][0]); acc[i][1] = mfma16(wa[st * 2 + i], bq[2 * st + 1], acc[i][1]); }
        if (st & 1) LB(12 + st / 2);
      }
    }
#pragma unroll
    for (int th = 0; th < 2; th++) { acc_row(v[th], acc, th);
#pragma unroll
      for (int e = 0; e < 8; e++) v[th][e] = silu_f(v[th][e]); }
    act_store(sm, L_ACT1, c, pack8(v[0]), pack8(v[1]));
    BAR();
    TS(27);
    zero_acc(acc);
    gemm_tiled(acc, wb, sm, L_ACT1, c);
#pragma unroll
    for (int th = 0; th < 2; th++) { acc_row(v[th], acc, th);
      const int dest = th * 2 + (c.tok >> 3);
      if (dest != c.s) {
        u32x4 o = pack8(v[th]); o = u32x4{san(o[0]), san(o[1]), san(o[2]), san(o[3])};
        st128(o, rws, dest * 16384 + ((c.w * 4 + c.g) * 8 + (c.tok & 7)) * 16, WS_P2 + (c.tg * 32 + c.s) * 4096);
      } else {
        float x[8]; unpack8(x, xr[th]);
        const int o = L_ACT0 + (c.tok & 7) * 1040 + (c.w * 32 + c.g * 4) * 4;
        *(f32x4*)(sm + o) = f32x4{fmaf(v[th][0], 0.5f, x[0]), fmaf(v[th][1], 0.5f, x[1]), fmaf(v[th][2], 0.5f, x[2]), fmaf(v[th][3], 0.5f, x[3])};
        *(f32x4*)(sm + o + 64) = f32x4{fmaf(v[th][4], 0.5f, x[4]), fmaf(v[th][5], 0.5f, x[5]), fmaf(v[th][6], 0.5f, x[6]), fmaf(v[th][7], 0.5f, x[7])};
      }
    }
  }
  TS(28);
  {
    const int tk = (c.lane >> 1) & 7, f0 = c.w * 32 + (c.lane & 1) * 16 + c.g * 4;
    asm volatile("s_waitcnt lgkmcnt(0)" ::: "memory");
    f32x4 x = *(f32x4*)(sm + L_ACT0 + tk * 1040 + f0 * 4);
    u32x2 pp[3];
    for (int sp = 0;; sp++) {
#pragma unroll
      for (int k = 0; k < 3; k++) pp[k] = __builtin_amdgcn_raw_buffer_load_b64(rws, c.tid * 8, WS_P2 + (c.tg * 32 + c.s * 4 + ((c.s + 1 + k) & 3)) * 4096, 16);
      unsigned mx = 0;
#pragma unroll
      for (int k = 0; k < 3; k++) mx = max(mx, max(pp[k][0], pp[k][1]));
      if (dead || !__any(mx == ~0u)) break;
      if (sp > SPIN_CAP) { dead = true; break; }
      __builtin_amdgcn_s_sleep(3);
      asm volatile("" ::: "memory");
    }
    TS(29);
    f32x4 a = {0.f, 0.f, 0.f, 0.f};
#pragma unroll
    for (int k = 0; k < 3; k++) a += f32x4{lo16(pp[k][0]), hi16(pp[k][0]), lo16(pp[k][1]), hi16(pp[k][1])};
    x += 0.5f * a;
    float t = swadd32(x[0] + x[1] + x[2] + x[3], x[0] * x[0] + x[1] * x[1] + x[2] * x[2] + x[3] * x[3]);
    t = swadd16(t, t);
    t += __uint_as_float(__builtin_amdgcn_update_dpp(0, __float_as_uint(t), 0xB1, 0xf, 0xf, false));
    *(float*)(sm + L_STAT_B + c.tid * 4) = t;
    BAR();
    float u = 0.f;
#pragma unroll
    for (int k = 0; k < 8; k++) u += *(float*)(sm + L_STAT_B + (k * 64 + c.lane) * 4);
    auto r = __builtin_amdgcn_permlane32_swap(__float_as_uint(u), __float_as_uint(u), false, false);
    const float mean = __uint_as_float(r[0]) * (1.f / 256.f);
    const float rstd = __builtin_amdgcn_rsqf(fmaxf(fmaf(-mean, mean, __uint_as_float(r[1]) * (1.f / 256.f)), 0.f) + 1e-5f);
    x = (x - mean) * rstd;
    const int tt = c.tile * 32 + c.s * 8 + tk;
    __builtin_amdgcn_raw_buffer_store_b64(u32x2{pack2(x[0], x[1]), pack2(x[2], x[3])}, mk_rsrc(p.out, 2000 * 512), ((c.b * T_LEN + tt) * 512 + f0 * 2) | (tt < T_LEN ? 0 : 0x40000000), 0, OUT_AUX);
  }
  TS(30); VMW(); TS(31); TS_OUT(0);
}

extern "C" void kernel_launch(void* const* d_in, const int* in_sizes, int n_in, void* d_out, int out_size, void* d_ws, size_t ws_size, hipStream_t stream) {
  Params p;
  p.x = (const u16*)d_in[0]; p.w1a = (const u16*)d_in[1]; p.w1b = (const u16*)d_in[2]; p.wqkv = (const u16*)d_in[3]; p.wo = (const u16*)d_in[4];
  p.wp1 = (const u16*)d_in[5]; p.dw = (const u16*)d_in[6]; p.wp2 = (const u16*)d_in[7]; p.w2a = (const u16*)d_in[8]; p.w2b = (const u16*)d_in[9];
  p.out = (u16*)d_out; p.ws = (char*)d_ws;
  k_all<<<256, 512, 0, stream>>>(p);
}
```

```cpp
#include <hip/hip_runtime.h>
#include <hip/hip_bf16.h>

typedef unsigned short u16;
typedef unsigned long long u64;
typedef __attribute__((ext_vector_type(2))) unsigned u32x2;
typedef __attribute__((ext_vector_type(4))) unsigned u32x4;
typedef __attribute__((ext_vector_type(2))) float f32x2;
typedef __attribute__((ext_vector_type(4))) float f32x4;
typedef __attribute__((ext_vector_type(16))) float f32x16;
typedef __attribute__((ext_vector_type(2))) __bf16 bf16x2;
typedef __attribute__((ext_vector_type(4))) __bf16 bf16x4;
typedef __attribute__((ext_vector_type(8))) __bf16 bf16x8;
typedef __attribute__((ext_vector_type(4))) short s16x4;
typedef __amdgpu_buffer_rsrc_t rsrc_t;
#define DI __device__ __forceinline__
#define LAS __attribute__((address_space(3)))
#define SB() __builtin_amdgcn_sched_barrier(0)
#define PIN(x) asm volatile("" : "+v"(x))

constexpr int T_LEN = 1000;
constexpr float LOG2E = 1.4426950408889634f;

constexpr unsigned WS_WT  = 0;
constexpr unsigned WS_SYNC = 1536u << 10;
constexpr unsigned WS_KV  = 2u << 20;
constexpr unsigned WS_G   = 4u << 20;
constexpr unsigned WS_ATT = 5u << 20;
constexpr unsigned WS_P1  = 6u << 20;
constexpr unsigned WS_P2  = WS_P1 + 65536;
constexpr unsigned WT_P2 = 0, WT_2A = 128u << 10, WT_2B = 640u << 10, WT_P1 = 1152u << 10, WT_DW = 1408u << 10;
constexpr unsigned SY_E = 0, SY_F1 = 1024, SY_F2 = SY_F1 + 131072;
constexpr unsigned WS_END = 14u << 20;
constexpr unsigned WS_PROF = 64u << 20;

constexpr int L_RING = 0, L_ACT0 = 131072, L_ACT1 = 147456, L_END = 163840;
constexpr int L_STAT_A = L_ACT1, L_Q = L_ACT1 + 4096, L_COMB = L_ACT0, L_ML = 122880, L_STAT_B = 124928, L_G = L_ACT0;
#define BAR() asm volatile("s_waitcnt lgkmcnt(0)\n\ts_barrier" ::: "memory")
#define VMCNT(n) asm volatile("s_waitcnt vmcnt(" #n ")" ::: "memory")
#define OUT_AUX 17
#define ROT 1

DI rsrc_t mk_rsrc(const void* p, unsigned bytes) { return __builtin_amdgcn_make_buffer_rsrc((void*)p, 0, bytes, 0x00020000); }
DI unsigned pack2(float a, float b) { bf16x2 v = {(__bf16)a, (__bf16)b}; return __builtin_bit_cast(unsigned, v); }
DI float lo16(unsigned u) { return __uint_as_float(u << 16); }
DI float hi16(unsigned u) { return __uint_as_float(u & 0xffff0000u); }
DI f32x4 mfma16(u32x4 a, u32x4 b, f32x4 c) { return __builtin_amdgcn_mfma_f32_16x16x32_bf16(__builtin_bit_cast(bf16x8, a), __builtin_bit_cast(bf16x8, b), c, 0, 0, 0); }
DI f32x16 mfma32(u32x4 a, u32x4 b, f32x16 c) { return __builtin_amdgcn_mfma_f32_32x32x16_bf16(__builtin_bit_cast(bf16x8, a), __builtin_bit_cast(bf16x8, b), c, 0, 0, 0); }
DI f32x4 mfma4(u32x2 a, u32x2 b, f32x4 c) { return __builtin_amdgcn_mfma_f32_4x4x4bf16_1k(__builtin_bit_cast(s16x4, a), __builtin_bit_cast(s16x4, b), c, 0, 0, 0); }
DI float silu_f(float x) { return x * __builtin_amdgcn_rcpf(1.f + __builtin_amdgcn_exp2f(-LOG2E * x)); }
DI unsigned silu2(float a, float b) {
  const f32x2 x = {a, b}, t = x * f32x2{-LOG2E, -LOG2E}, d = f32x2{__builtin_amdgcn_exp2f(t[0]), __builtin_amdgcn_exp2f(t[1])} + f32x2{1.f, 1.f};
  const f32x2 y = x * f32x2{__builtin_amdgcn_rcpf(d[0]), __builtin_amdgcn_rcpf(d[1])};
  return pack2(y[0], y[1]);
}
DI float sigm_f(float x) { return __builtin_amdgcn_rcpf(1.f + __builtin_amdgcn_exp2f(-LOG2E * x)); }
DI u32x4 lds_r128(char* sm, int off) { return *(u32x4*)(sm + off); }
DI void lds_w128(char* sm, int off, u32x4 v) { *(u32x4*)(sm + off) = v; }
DI float swadd32(float a, float b) { auto r = __builtin_amdgcn_permlane32_swap(__float_as_uint(a), __float_as_uint(b), false, false); return __uint_as_float(r[0]) + __uint_as_float(r[1]); }
DI float swadd16(float a, float b) { auto r = __builtin_amdgcn_permlane16_swap(__float_as_uint(a), __float_as_uint(b), false, false); return __uint_as_float(r[0]) + __uint_as_float(r[1]); }

struct Ctx { int tid, w, lane, tok, g, b, tile, s, tg; };
DI Ctx mk_ctx() {
  Ctx c; c.tid = threadIdx.x; c.w = __builtin_amdgcn_readfirstlane(threadIdx.x >> 6); c.lane = c.tid & 63; c.tok = c.lane & 15; c.g = c.lane >> 4;
  int bid = blockIdx.x; c.s = (bid >> 3) & 3; c.tg = (bid & 7) * 8 + (bid >> 5); c.b = c.tg >> 5; c.tile = c.tg & 31; return c;
}

DI u32x4 tr_frag(const unsigned* r8, int i) {
  const unsigned sel = i ? 0x07060302u : 0x05040100u;
  u32x4 f;
#pragma unroll
  for (int m = 0; m < 4; m++) f[m] = __builtin_amdgcn_perm(r8[2 * m + 1], r8[2 * m], sel);
  return f;
}
DI void load_tiled(u32x4 (&fr)[16], rsrc_t rs, int voff, int soff) {
#pragma unroll
  for (int q = 0; q < 16; q++) fr[q] = __builtin_amdgcn_raw_buffer_load_b128(rs, voff + (q & 3) * 1024, soff + (q >> 2) * 4096, 16);
}
DI void st128(u32x4 v, rsrc_t rs, unsigned voff, unsigned soff) { __builtin_amdgcn_raw_buffer_store_b128(v, rs, voff + soff, 0, 16); }
DI void dma16(rsrc_t rs, int voff, int soff, int lds) {
  asm volatile("s_mov_b32 m0, %0\n\ts_nop 0\n\tbuffer_load_dwordx4 %1, %2, %3 offen lds" :: "s"(lds), "v"(voff), "s"(rs), "s"(soff) : "memory");
}
DI void dma16s(rsrc_t rs, int voff, int soff, int lds) {
  asm volatile("s_mov_b32 m0, %0\n\ts_nop 0\n\tbuffer_load_dwordx4 %1, %2, %3 offen sc1 lds" :: "s"(lds), "v"(voff), "s"(rs), "s"(soff) : "memory");
}
DI unsigned touch(rsrc_t rs, unsigned voff, bool on = true) { unsigned r; voff |= on ? 0u : 0x40000000u; asm volatile("buffer_load_dword %0, %1, %2, 0 offen sc1" : "=&v"(r) : "v"(voff), "s"(rs) : "memory"); return r; }
DI u32x4 tr_pair(char* sm, int o0, int o1) {
  u32x2 lo = __builtin_bit_cast(u32x2, __builtin_amdgcn_ds_read_tr16_b64_v4bf16((LAS bf16x4*)(sm + o0)));
  u32x2 hi = __builtin_bit_cast(u32x2, __builtin_amdgcn_ds_read_tr16_b64_v4bf16((LAS bf16x4*)(sm + o1)));
  return u32x4{lo[0], lo[1], hi[0], hi[1]};
}
DI void gemm_tiled(f32x4 (&acc)[2][2], const u32x4 (&fr)[16], char* sm, int act, const Ctx& c) {
  const int base = act + (c.g * 32 + c.tok) * 16;
#pragma unroll
  for (int st = 0; st < 8; st++) {
    u32x4 b0 = lds_r128(sm, base + st * 2048), b1 = lds_r128(sm, base + st * 2048 + 256);
#pragma unroll
    for (int i = 0; i < 2; i++) {
      acc[i][0] = mfma16(fr[st * 2 + i], b0, acc[i][0]);
      acc[i][1] = mfma16(fr[st * 2 + i], b1, acc[i][1]);
    }
  }
}
DI void zero_acc(f32x4 (&acc)[2][2]) {
#pragma unroll
  for (int i = 0; i < 2; i++)
#pragma unroll
    for (int t = 0; t < 2; t++) acc[i][t] = f32x4{0.f, 0.f, 0.f, 0.f};
}
DI void unpack8(float (&v)[8], u32x4 p) {
#pragma unroll
  for (int m = 0; m < 4; m++) { v[2 * m] = lo16(p[m]); v[2 * m + 1] = hi16(p[m]); }
}
DI u32x4 pack8(const float (&v)[8]) { return u32x4{pack2(v[0], v[1]), pack2(v[2], v[3]), pack2(v[4], v[5]), pack2(v[6], v[7])}; }
DI void act_store(char* sm, int act, const Ctx& c, u32x4 p0, u32x4 p1) {
  const int o = act + ((c.w * 4 + c.g) * 32 + c.tok) * 16;
  lds_w128(sm, o, p0); lds_w128(sm, o + 256, p1);
}
DI void ln_part1(const u32x4 (&xr)[2], char* sm, int stat, const Ctx& c) {
  float s0 = 0, s1 = 0, q0 = 0, q1 = 0;
  unsigned ou = 0x3f803f80u; PIN(ou);
  const bf16x2 one = __builtin_bit_cast(bf16x2, ou);
#pragma unroll
  for (int m = 0; m < 4; m++) { const unsigned ua = xr[0][m], ub = xr[1][m]; const bf16x2 a = __builtin_bit_cast(bf16x2, ua), b = __builtin_bit_cast(bf16x2, ub);
    s0 = __builtin_amdgcn_fdot2_f32_bf16(a, one, s0, false); s1 = __builtin_amdgcn_fdot2_f32_bf16(b, one, s1, false);
    q0 = __builtin_amdgcn_fdot2_f32_bf16(a, a, q0, false); q1 = __builtin_amdgcn_fdot2_f32_bf16(b, b, q1, false); }
  float t = swadd16(swadd32(s0, s1), swadd32(q0, q1));
  *(float*)(sm + stat + (c.w * 64 + c.lane) * 4) = t;
}
DI void ln_part2(const u32x4 (&xr)[2], char* sm, int act, int stat, const Ctx& c) {
  float v[2][8]; unpack8(v[0], xr[0]); unpack8(v[1], xr[1]);
  float u = 0;
#pragma unroll
  for (int k = 0; k < 8; k++) u += *(float*)(sm + stat + (k * 64 + c.lane) * 4);
  auto r = __builtin_amdgcn_permlane16_swap(__float_as_uint(u), __float_as_uint(u), false, false);
  float mean = __uint_as_float(r[0]) * (1.f / 256.f);
  float var = fmaxf(fmaf(-mean, mean, __uint_as_float(r[1]) * (1.f / 256.f)), 0.f);
  float rstd = __builtin_amdgcn_rsqf(var + 1e-5f), nm = -mean * rstd;
  auto rr = __builtin_amdgcn_permlane32_swap(__float_as_uint(rstd), __float_as_uint(rstd), false, false);
  auto rn = __builtin_amdgcn_permlane32_swap(__float_as_uint(nm), __float_as_uint(nm), false, false);
#pragma unroll
  for (int t2 = 0; t2 < 2; t2++)
#pragma unroll
    for (int e = 0; e < 8; e += 2) { const float r_ = __uint_as_float(rr[t2]), n_ = __uint_as_float(rn[t2]);
      const f32x2 z = __builtin_elementwise_fma(f32x2{v[t2][e], v[t2][e + 1]}, f32x2{r_, r_}, f32x2{n_, n_}); v[t2][e] = z[0]; v[t2][e + 1] = z[1]; }
  act_store(sm, act, c, pack8(v[0]), pack8(v[1]));
}
DI void acc_row(float (&v)[8], const f32x4 (&acc)[2][2], int th) {
#pragma unroll
  for (int j = 0; j < 4; j++) { v[j] = acc[0][th][j]; v[4 + j] = acc[1][th][j]; }
}
struct Params { const u16 *x, *w1a, *w1b, *wqkv, *wo, *wp1, *dw, *wp2, *w2a, *w2b; u16* out; char* ws; };

DI unsigned ald16(rsrc_t rs, int voff) { unsigned r; asm volatile("buffer_load_ushort %0, %1, %2, 0 offen" : "=&v"(r) : "v"(voff), "s"(rs) : "memory"); return r; }
DI void prep_issue(const Params& p, int task, int lane, int lds, unsigned (&raw)[8]) {
  if (task < 352) {
    const int st = task & 7, r8 = lane >> 3, ck = lane & 7;
    const u16* src; int vo, so, step; unsigned bytes;
    if (task < 32) { src = p.wp2; bytes = 131072; vo = ((r8 >> 2) * 8 + (r8 & 3)) * 512 + ck * 16; so = st * 32 * 512 + (task >> 3) * 128; step = 0; }
    else if (task < 160) { src = p.w2a; bytes = 524288; vo = r8 * 2048 + ck * 16; so = st * 32 * 2048 + ((task - 32) >> 3) * 128; step = 8 * 2048; }
    else if (task < 288) { const int u = (task - 160) >> 3; src = p.w2b; bytes = 524288; vo = r8 * 512 + ck * 16; so = ((u >> 2) * 256 + st * 32) * 512 + (u & 3) * 128; step = 8 * 512; }
    else { src = p.wp1; bytes = 262144; vo = r8 * 1024 + (ck >> 1) * 16 + (ck & 1) * 512; so = st * 32 * 1024 + ((task - 288) >> 3) * 64; step = 8 * 1024; }
    rsrc_t rs = mk_rsrc(src, bytes);
#pragma unroll
    for (int i = 0; i < 4; i++) dma16(rs, vo + so + (task < 32 ? ((i & 1) * 16 + (i >> 1) * 4) * 512 : i * step), 0, lds + i * 1024);
  } else {
    const int t = task - 352, grp = t / 5, m = t - grp * 5;
    const int ch = (16 * grp + (lane >> 2)) & 255, i = lane & 3;
    rsrc_t rs = mk_rsrc(p.dw, 31 * 512);
#pragma unroll
    for (int e = 0; e < 8; e++) raw[e] = ald16(rs, (unsigned)(8 * m + e - 1 - i) < 31u ? (8 * m + e - 1 - i) * 512 + ch * 2 : 0x100000);
  }
}
DI void prep_finish(int task, int lane, char* sm, int lds, const unsigned (&raw)[8], rsrc_t rws) {
  if (task < 352) {
    const int st = task & 7, tb = lds + ((lane >> 4) * 4 + ((lane & 15) >> 2)) * 128 + (lane & 3) * 8;
    const unsigned d = task < 288 ? WS_WT + WT_P2 + (task >> 3) * 32768 + st * 2048 : WS_WT + WT_P1 + ((task - 288) >> 3) * 32768 + st * 1024;
#pragma unroll
    for (int i = 0; i < 4; i++) st128(tr_pair(sm, tb + i * 32, tb + i * 32 + 2048), rws, lane * 16, d + (task < 288 ? (i >> 1) * 16384 + (i & 1) * 1024 : i * 8192));
  } else st128(tr_frag(raw, 0), rws, lane * 16, task < 432 ? WS_WT + WT_DW + (task - 352) * 1024 : 0x40000000u);
}

#define TS_DECL
#define TS(i)
#define TS_OUT(k)
#define VMW()
#define KATTR __global__ __launch_bounds__(512) __attribute__((amdgpu_waves_per_eu(2, 2)))

constexpr int PF_CODE = 27 * 1024 + 512;
constexpr int SPIN_CAP = 1 << 17;
#define DRAIN() asm volatile("s_waitcnt vmcnt(0)" ::: "memory")
DI u32x2 ld_flag(rsrc_t rws, int off) { return __builtin_amdgcn_raw_buffer_load_b64(rws, off, WS_SYNC, 16); }
DI void st_flag(rsrc_t rws, int off, u32x2 v) { __builtin_amdgcn_raw_buffer_store_b64(v, rws, off, WS_SYNC, 16); }
DI void poll_flags(rsrc_t rws, int off, u32x2 ep, bool& dead) {
  if (dead) return;
  for (int sp = 0;; sp++) {
    u32x2 f = ld_flag(rws, off);
    if (__all(f[0] == ep[0] && f[1] == ep[1])) break;
    if (sp > SPIN_CAP) { dead = true; break; }
    __builtin_amdgcn_s_sleep(2);
    asm volatile("" ::: "memory");
  }
  asm volatile("" ::: "memory");
}
DI void poll_flags(rsrc_t rws, int off, u32x2 ep, bool& dead, u32x2 first) { if (!__all(first[0] == ep[0] && first[1] == ep[1])) poll_flags(rws, off, ep, dead); asm volatile("" ::: "memory"); }
DI unsigned umax4(u32x4 v) { return max(max(v[0], v[1]), max(v[2], v[3])); }
DI unsigned san(unsigned v) { return min(v, 0xFFFFFFFEu); }

KATTR void k_all(Params p) {
  __shared__ __attribute__((aligned(16))) char sm[L_END];
  TS_DECL; TS(0);
  unsigned pt, pt0;
  const int jx = blockIdx.x >> 3;
  {
    const char* q = (const char*)(__builtin_amdgcn_s_getpc() & ~127ull) + min(jx * 8 + (int)(threadIdx.x >> 6), PF_CODE / 128 - 1) * 128;
    asm volatile("global_load_dword %0, %1, off" : "=&v"(pt) : "v"(q) : "memory");
  }
  const Ctx c = mk_ctx();
  rsrc_t rws = mk_rsrc(p.ws, WS_END);
  const unsigned wt = WS_WT;
  bool dead = false;
  u32x2 ep;
  u32x4 xr[2];
  f32x4 acc[2][2];
  float v[2][8];
  const int bbase = (c.g * 32 + c.tok) * 16;
    const int rot = ROT ? (c.tg & 3) : 0;
    rsrc_t rx = mk_rsrc(p.x, 2000 * 512), ra = mk_rsrc(p.w1a, 524288), rb = mk_rsrc(p.w1b, 524288), rq = mk_rsrc(p.wqkv, 393216), ro = mk_rsrc(p.wo, 131072);
#pragma unroll
    for (int i = 0; i < 2; i++) {
      const int row = c.w * 4 + i * 2 + (c.lane >> 5), t = c.tile * 32 + row;
      dma16(rx, ((c.b * T_LEN + t) * 512 + (((c.lane & 31) ^ (row & 15)) * 16)) | (t < T_LEN ? 0 : 0x40000000), 0, L_ACT0 + c.w * 2048 + i * 1024);
    }
    int va[4], vb[4];
#pragma unroll
    for (int i = 0; i < 4; i++) { const int row = 2 * i + (c.lane >> 5), ch = (c.lane & 31) ^ (row << 1); va[i] = row * 2048 + ch * 16; vb[i] = row * 512 + ch * 16; }
    const int vq = (c.lane >> 3) * 1536 + (((c.lane & 7) ^ (((c.lane >> 4) & 3) << 1)) * 16);
    auto dma_a = [&](int k) { const int kc = (k + rot) & 3;
#pragma unroll
      for (int i = 0; i < 4; i++) dma16(ra, va[i], (kc * 64 + c.w * 8) * 2048 + c.s * 512, L_RING + k * 32768 + c.w * 4096 + i * 1024); };
    auto dma_b = [&](int k) { const int kc = (k + rot) & 3;
#pragma unroll
      for (int i = 0; i < 4; i++) dma16(rb, vb[i], (c.s * 256 + kc * 64 + c.w * 8) * 512, L_RING + k * 32768 + c.w * 4096 + i * 1024); };
    auto dma_q = [&](int seg) {
#pragma unroll
      for (int i = 0; i < 4; i++) dma16(rq, vq, (c.w * 32 + i * 8) * 1536 + seg * 512 + c.s * 128, L_RING + seg * 32768 + c.w * 4096 + i * 1024); };
    const int trow = c.g * 4 + (c.tok >> 2), tp = c.tok & 3;
    const int tb0 = L_RING + trow * 512 + (((c.w * 4 + (tp >> 1)) ^ ((trow & 7) << 1)) * 16) + (tp & 1) * 8, tb1 = tb0 ^ 32;
    u32x4 fr[16];
    auto tr_chunk = [&](int k) {
#pragma unroll
      for (int h = 0; h < 2; h++) { const int o = k * 32768 + h * 16384;
        fr[(2 * k + h) * 2] = tr_pair(sm, tb0 + o, tb0 + o + 8192); fr[(2 * k + h) * 2 + 1] = tr_pair(sm, tb1 + o, tb1 + o + 8192); } };
    u32x4 bb[16];
    auto act_rd = [&](int k, int act) { const int o = act + bbase + ((k + rot) & 3) * 4096;
#pragma unroll
      for (int h = 0; h < 2; h++) { bb[k * 4 + h * 2] = lds_r128(sm, o + h * 2048); bb[k * 4 + h * 2 + 1] = lds_r128(sm, o + h * 2048 + 256); } };
    auto mm = [&](int q) {
#pragma unroll
      for (int i = 0; i < 2; i++) { acc[i][0] = mfma16(fr[q * 2 + i], bb[2 * q], acc[i][0]); acc[i][1] = mfma16(fr[q * 2 + i], bb[2 * q + 1], acc[i][1]); } };
    auto dma_b1 = [&](int k, int i) { dma16(rb, vb[i], (c.s * 256 + ((k + rot) & 3) * 64 + c.w * 8) * 512, L_RING + k * 32768 + c.w * 4096 + i * 1024); };
    auto dma_q1 = [&](int seg, int i) { dma16(rq, vq, (c.w * 32 + i * 8) * 1536 + seg * 512 + c.s * 128, L_RING + seg * 32768 + c.w * 4096 + i * 1024); };
    dma_a(0); dma_a(1); dma_a(2);
    TS(1);
    asm volatile("s_waitcnt vmcnt(12)" : "+v"(pt) :: "memory");
    BAR();
#pragma unroll
    for (int th = 0; th < 2; th++) { const int o = L_ACT0 + (th * 16 + c.tok) * 512 + (c.g & 1) * 8, ck = c.w * 4 + (c.g >> 1);
      const u32x2 lo = *(u32x2*)(sm + o + ((ck ^ c.tok) * 16)), hi = *(u32x2*)(sm + o + (((ck + 2) ^ c.tok) * 16));
      xr[th] = u32x4{lo[0], lo[1], hi[0], hi[1]}; }
    dma_a(3);
    ln_part1(xr, sm, L_STAT_A, c);
    VMCNT(8); BAR();
    {
      const char* q = c.w == 0 ? (const char*)p.w1b + c.s * 131072 : c.w == 1 ? (const char*)p.w1b + c.s * 131072 + 65536 : c.w == 2 ? (const char*)p.wqkv : c.w == 3 ? (const char*)p.wo
        : c.w == 4 ? (const char*)p.out + (c.b * T_LEN + c.tile * 32) * 512 : p.ws + (c.w == 5 ? WS_P1 + c.tg * 131072 : c.w == 6 ? WS_KV : WS_G);
      asm volatile("global_load_dword %0, %1, off" : "=&v"(pt0) : "v"(q) : "memory");
    }
    asm volatile("s_nop 4\n\tbuffer_load_dwordx2 %0, %1, %2, 0 offen sc1" : "=&v"(ep) : "v"(WS_SYNC + SY_E), "s"(rws) : "memory");
    tr_chunk(0); tr_chunk(1);
    ln_part2(xr, sm, L_ACT0, L_STAT_A, c);
    VMCNT(2); BAR();
    TS(2);
    act_rd(0, L_ACT0); act_rd(1, L_ACT0); SB(); tr_chunk(2); tr_chunk(3); act_rd(2, L_ACT0); act_rd(3, L_ACT0);
    zero_acc(acc);
#pragma unroll
    for (int q = 0; q < 4; q++) { dma_b1(0, q); mm(q); SB(); }
    BAR();
#pragma unroll
    for (int q = 0; q < 4; q++) { dma_b1(1, q); mm(4 + q); SB(); }
    u32x4 hp[2];
#pragma unroll
    for (int th = 0; th < 2; th++) { acc_row(v[th], acc, th);
#pragma unroll
      for (int e = 0; e < 4; e++) { dma_b1(2 + th, e); unsigned u = silu2(v[th][2 * e], v[th][2 * e + 1]); PIN(u); hp[th][e] = u; } }
    act_store(sm, L_ACT1, c, hp[0], hp[1]);
    {
      const int t = (c.w - 6) * 256 + blockIdx.x, r = (t & 7) * 32 + (c.lane & 31);
      const char* q = c.w < 6 ? (const char*)p.wqkv + ((jx >> 2) * 32 + (c.w & 1) * 16 + c.tok) * 1536 + (c.w >> 1) * 512 + c.s * 128
        : t < 32 ? (const char*)p.wp2 + r * 512 + (t >> 3) * 128 : t < 160 ? (const char*)p.w2a + r * 2048 + ((t - 32) >> 3) * 128 : t < 288 ? (const char*)p.w2b + ((((t - 160) >> 5) * 256 + r) * 512 + (((t - 160) >> 3) & 3) * 128)
        : t < 352 ? (const char*)p.wp1 + r * 1024 + ((t - 288) >> 3) * 64 + (c.lane >> 5) * 512 : (const char*)p.dw + min(c.lane & 31, 30) * 512 + ((t - 352) / 5 & 15) * 32;
      asm volatile("global_load_dword %0, %1, off" : "=&v"(pt) : "v"(q) : "memory");
    }
    asm volatile("s_waitcnt vmcnt(9)" : "+v"(ep), "+v"(pt0) :: "memory");
    BAR();
    { u64 e = (((u64)ep[1] << 32) | ep[0]) + 1; ep[0] = __builtin_amdgcn_readfirstlane((unsigned)e); ep[1] = __builtin_amdgcn_readfirstlane((unsigned)(e >> 32)); }
    TS(3);
    tr_chunk(0); act_rd(0, L_ACT1); SB(); tr_chunk(1); act_rd(1, L_ACT1); SB(); act_rd(2, L_ACT1); act_rd(3, L_ACT1);
    zero_acc(acc);
    mm(0); mm(1); mm(2); mm(3); SB();
    VMCNT(1); BAR();
    tr_chunk(2); tr_chunk(3);
#pragma unroll
    for (int q = 0; q < 4; q++) { dma_q1(0, q); mm(4 + q); SB(); }
  u32x4 own[2];
#pragma unroll
  for (int th = 0; th < 2; th++) { acc_row(v[th], acc, th); own[th] = pack8(v[th]);
    st128(own[th], rws, c.tid * 16 + th * 8192, WS_P1 + (c.tg * 8 + c.s) * 16384); }
  TS(4);
  dma_q(1);
  asm volatile("s_waitcnt vmcnt(4)" : "+v"(pt) :: "memory");
  st_flag(rws, c.lane ? 0x40000000 : SY_F1 + ((c.tg * 4 + c.s) * 8 + c.w) * 64, ep);
  BAR();
  dma_q(2);
  unsigned raw[8];
  const int task = (c.w - 6) * 256 + blockIdx.x, plds = L_RING + 3 * 32768 + (c.w & 1) * 4096;
  if (c.w >= 6) prep_issue(p, task, c.lane, plds, raw);
  else pt = touch(ro, (jx * 32 + c.lane) * 128, (c.tid >> 5) == 10);
  TS(5);
  {
    poll_flags(rws, SY_F1 + ((c.tg * 4 + (c.lane & 3)) * 8 + c.w) * 64, ep, dead);
    TS(7);
    u32x4 pp[3][2];
#pragma unroll
    for (int k = 0; k < 3; k++)
#pragma unroll
      for (int th = 0; th < 2; th++) asm volatile("buffer_load_dwordx4 %0, %1, %2, %3 offen sc1" : "=&v"(pp[k][th]) : "v"(c.tid * 16 + th * 8192), "s"(rws), "s"(WS_P1 + (c.tg * 8 + ((c.s + 1 + k) & 3)) * 16384) : "memory");
    BAR();
    u32x4 wq[16];
    {
      const int ws_ = c.w < 4 ? c.w + 2 : (c.w & 1);
      const int trow = c.g * 4 + (c.tok >> 2), tp = c.tok & 3;
      const int tb0 = L_RING + (ws_ >> 1) * 32768 + trow * 128 + ((((ws_ & 1) * 4 + (tp >> 1)) ^ (((trow >> 1) & 3) << 1)) * 16) + (tp & 1) * 8, tb1 = tb0 ^ 32;
#pragma unroll
      for (int st = 0; st < 8; st++) { wq[2 * st] = tr_pair(sm, tb0 + st * 4096, tb0 + st * 4096 + 2048); wq[2 * st + 1] = tr_pair(sm, tb1 + st * 4096, tb1 + st * 4096 + 2048); }
    }
    if (c.w >= 6) {
      asm volatile("" : "+v"(raw[0]), "+v"(raw[1]), "+v"(raw[2]), "+v"(raw[3]), "+v"(raw[4]), "+v"(raw[5]), "+v"(raw[6]), "+v"(raw[7]) :: "memory");
      prep_finish(task, c.lane, sm, plds, raw, rws);
      if (task < 352) VMCNT(4); else VMCNT(1);
    } else asm volatile("s_waitcnt vmcnt(0)" : "+v"(pt) :: "memory");
    asm volatile("" : "+v"(pp[0][0]), "+v"(pp[0][1]), "+v"(pp[1][0]), "+v"(pp[1][1]), "+v"(pp[2][0]), "+v"(pp[2][1]) :: "memory");
    SB(); TS(8);
#pragma unroll
    for (int th = 0; th < 2; th++) {
      float x[8], a[8], t[8]; unpack8(x, xr[th]); unpack8(a, own[th]);
#pragma unroll
      for (int k = 0; k < 3; k++) { unpack8(t, pp[k][th]);
#pragma unroll
        for (int e = 0; e < 8; e++) a[e] += t[e]; }
#pragma unroll
      for (int e = 0; e < 8; e++) x[e] = fmaf(a[e], 0.5f, x[e]);
      xr[th] = pack8(x);
    }
    TS(9);
    ln_part1(xr, sm, L_STAT_A, c);
    {
      const u32x4 ff = {~0u, ~0u, ~0u, ~0u};
      st128(ff, rws, c.lane * 16, c.w < 4 ? WS_ATT + (c.tg * 4 + c.s) * 4096 + c.w * 1024 : WS_G + (c.b * 64 + c.tile * 2 + ((c.w >> 1) & 1)) * 8192 + c.s * 2048 + (c.w & 1) * 1024);
    }
    BAR();
    dma16(ro, ((c.w & 1) ? vb[2] : vb[0]), (28 + (c.w >> 1)) * 4096, L_RING + (28 + (c.w >> 1)) * 4096 + (c.w & 1) * 2048);
    dma16(ro, ((c.w & 1) ? vb[3] : vb[1]), (28 + (c.w >> 1)) * 4096, L_RING + (28 + (c.w >> 1)) * 4096 + (c.w & 1) * 2048 + 1024);
    ln_part2(xr, sm, L_ACT0, L_STAT_A, c); BAR();
    TS(10);
    zero_acc(acc);
    const unsigned kvb = WS_KV + ((c.b * 4 + c.s) * 32 + c.tile) * 8192;
    if (c.w < 6 && (c.w & 2) == 0) {
      gemm_tiled(acc, wq, sm, L_ACT0, c);
#pragma unroll
      for (int th = 0; th < 2; th++) { acc_row(v[th], acc, th);
        const int o = (((c.w & 1) * 4 + c.g) * 32 + th * 16 + c.tok) * 16;
        if (c.w >= 4) lds_w128(sm, L_Q + o, pack8(v[th]));
        else st128(pack8(v[th]), rws, o, kvb); }
    } else if (c.w < 6) {
#pragma unroll
      for (int st = 0; st < 8; st++) {
        u32x4 b0 = lds_r128(sm, L_ACT0 + bbase + st * 2048), b1 = lds_r128(sm, L_ACT0 + bbase + st * 2048 + 256);
#pragma unroll
        for (int i = 0; i < 2; i++) { acc[i][0] = mfma16(b0, wq[st * 2 + i], acc[i][0]); acc[i][1] = mfma16(b1, wq[st * 2 + i], acc[i][1]); }
      }
#pragma unroll
      for (int th = 0; th < 2; th++) {
        auto r0 = __builtin_amdgcn_permlane32_swap(pack2(acc[0][th][0], acc[0][th][1]), pack2(acc[1][th][0], acc[1][th][1]), false, false);
        auto r1 = __builtin_amdgcn_permlane32_swap(pack2(acc[0][th][2], acc[0][th][3]), pack2(acc[1][th][2], acc[1][th][3]), false, false);
        u32x4 o = {r0[0], r1[0], r0[1], r1[1]};
        st128(o, rws, (8 * (c.tok >> 2) + 4 * (c.lane >> 5) + (c.tok & 3) + 32 * (c.g & 1)) * 16 + ((c.w & 1) * 2 + th) * 1024, kvb + 4096);
      }
    } else {
#pragma unroll 1
      for (int n = 0; n < 7; n++) { const int o = (n * 2 + (c.w & 1)) * 4096;
#pragma unroll
        for (int i = 0; i < 4; i++) dma16(ro, vb[i], o, L_RING + o + i * 1024); }
    }
  }
  TS(11);
  if (c.w < 6) DRAIN(); else VMCNT(28);
  BAR();
  if (c.w == 0) st_flag(rws, c.lane ? 0x40000000 : SY_F2 + (c.tg * 4 + c.s) * 128, ep);
  TS(12);
  u32x4 wo[16], w1[8];
  {
    u32x4 qf[4], kf[4][4], vf[4][4];
#pragma unroll
    for (int ks = 0; ks < 4; ks++) qf[ks] = lds_r128(sm, L_Q + c.lane * 16 + ks * 1024);
    wo[14] = tr_pair(sm, tb0 + 7 * 16384, tb0 + 7 * 16384 + 8192); wo[15] = tr_pair(sm, tb1 + 7 * 16384, tb1 + 7 * 16384 + 8192);
    BAR();
    if (c.w < 7) {
#pragma unroll
      for (int n = 0; n < 8; n++) dma16(ro, vb[n & 3], (14 + c.w * 2 + (n >> 2)) * 4096, L_RING + (14 + c.w * 2) * 4096 + n * 1024);
    }
    poll_flags(rws, SY_F2 + ((c.b * 32 + c.w * 4 + (c.lane & 3)) * 4 + c.s) * 128, ep, dead);
    TS(14);
    const unsigned kvb = WS_KV + ((c.b * 4 + c.s) * 32 + c.w * 4) * 8192;
    __builtin_amdgcn_s_setprio(2);
#pragma unroll
    for (int t = 0; t < 4; t++)
#pragma unroll
      for (int ks = 0; ks < 4; ks++) kf[t][ks] = __builtin_amdgcn_raw_buffer_load_b128(rws, c.lane * 16 + ks * 1024, kvb + t * 8192, 16);
    SB();
    __builtin_amdgcn_s_setprio(1);
    TS(13);
    f32x16 S[4];
    auto ldv = [&](int t) { SB();
#pragma unroll
      for (int ks = 0; ks < 4; ks++) vf[t][ks] = __builtin_amdgcn_raw_buffer_load_b128(rws, c.lane * 16 + ks * 1024, kvb + t * 8192 + 4096, 16);
      SB(); };
#pragma unroll
    for (int t = 0; t < 4; t++) {
#pragma unroll
      for (int e = 0; e < 16; e++) S[t][e] = 0.f;
#pragma unroll
      for (int ks = 0; ks < 4; ks++) S[t] = mfma32(kf[t][ks], qf[ks], S[t]);
    }
    ldv(0);
    __builtin_amdgcn_s_setprio(0);
    TS(15);
    if (c.w == 7) {
#pragma unroll
      for (int e = 4; e < 16; e++) S[3][e] = -INFINITY;
    }
    float m = S[0][0];
#pragma unroll
    for (int t = 0; t < 4; t++)
#pragma unroll
      for (int e = 0; e < 16; e++) m = fmaxf(m, S[t][e]);
    { auto r = __builtin_amdgcn_permlane32_swap(__float_as_uint(m), __float_as_uint(m), false, false); m = fmaxf(__uint_as_float(r[0]), __uint_as_float(r[1])); }
    const float CS = 0.125f * LOG2E;
    float mc = -m * CS; PIN(mc);
    f32x2 l2 = {0.f, 0.f};
    f32x16 O[2];
#pragma unroll
    for (int e = 0; e < 16; e++) { O[0][e] = 0.f; O[1][e] = 0.f; }
    u32x4 pf[4][2]; u32x2 fl;
    auto pv = [&](int t) {
#pragma unroll
      for (int s2 = 0; s2 < 2; s2++) { O[0] = mfma32(vf[t][s2], pf[t][s2], O[0]); O[1] = mfma32(vf[t][2 + s2], pf[t][s2], O[1]); } };
#pragma unroll
    for (int t = 0; t < 4; t++) {
      if (t < 3) ldv(t + 1);
      else { SB();
        fl = ld_flag(rws, SY_F2 + ((((c.lane >> 4) & 1) * 32 + c.w * 4) * 4 + (c.lane & 15)) * 128);
        SB(); }
      if (t >= 2) pv(t - 2);
#pragma unroll
      for (int e = 0; e < 16; e += 2) {
        f32x2 z = __builtin_elementwise_fma(f32x2{S[t][e], S[t][e + 1]}, f32x2{CS, CS}, f32x2{mc, mc});
        z = f32x2{__builtin_amdgcn_exp2f(z[0]), __builtin_amdgcn_exp2f(z[1])}; l2 += z; pf[t][e >> 3][(e >> 1) & 3] = pack2(z[0], z[1]); }
      PIN(pf[t][0]); PIN(pf[t][1]); PIN(l2);
    }
    SB(); pv(2); pv(3);
    float l = l2[0] + l2[1]; l = swadd32(l, l);
    if (c.lane < 32) *(f32x2*)(sm + L_ML + (c.w * 32 + c.lane) * 8) = f32x2{m, l};
    const float rl = __builtin_amdgcn_rcpf(l);
#pragma unroll
    for (int dt = 0; dt < 2; dt++)
#pragma unroll
      for (int q = 0; q < 4; q++) *(u32x2*)(sm + L_COMB + ((c.w * 8 + dt * 4 + q) * 64 + c.lane) * 8) = u32x2{pack2(O[dt][4 * q] * rl, O[dt][4 * q + 1] * rl), pack2(O[dt][4 * q + 2] * rl, O[dt][4 * q + 3] * rl)};
    poll_flags(rws, SY_F2 + ((((c.lane >> 4) & 1) * 32 + c.w * 4) * 4 + (c.lane & 15)) * 128, ep, dead, fl);
    TS(16);
    BAR();
    if (c.w == 0 && blockIdx.x == 0) st_flag(rws, c.lane ? 0x40000000 : SY_E, ep);
    TS(17);
    f32x2 ml[8]; u32x2 ok[8]; float M = -INFINITY;
#pragma unroll
    for (int k = 0; k < 8; k++) ml[k] = *(f32x2*)(sm + L_ML + (k * 32 + (c.lane & 31)) * 8);
#pragma unroll
    for (int k = 0; k < 8; k++) ok[k] = *(u32x2*)(sm + L_COMB + ((k * 8 + c.w) * 64 + c.lane) * 8);
    SB();
#pragma unroll
    for (int q = 0; q < 3; q++) w1[q] = __builtin_amdgcn_raw_buffer_load_b128(rws, c.lane * 16 + (q & 3) * 1024, wt + WT_P1 + (c.s * 8 + c.w) * 8192 + (q >> 2) * 4096, 16);
    SB();
#pragma unroll
    for (int k = 0; k < 8; k++) M = fmaxf(M, ml[k][0]);
    float L = 0.f; f32x4 o = {0.f, 0.f, 0.f, 0.f};
#pragma unroll
    for (int k = 0; k < 8; k++) {
      const float f = __builtin_amdgcn_exp2f((ml[k][0] - M) * CS) * ml[k][1];
      L += f;
      o += f * f32x4{lo16(ok[k][0]), hi16(ok[k][0]), lo16(ok[k][1]), hi16(ok[k][1])};
    }
    o *= __builtin_amdgcn_rcpf(L);
    __builtin_amdgcn_raw_buffer_store_b64(u32x2{san(pack2(o[0], o[1])), san(pack2(o[2], o[3]))}, rws, (c.w * 32 + (c.lane & 31)) * 16 + (c.lane >> 5) * 8, WS_ATT + (c.tg * 4 + c.s) * 4096, 16);
    SB();
#pragma unroll
    for (int st = 0; st < 7; st++) { wo[2 * st] = tr_pair(sm, tb0 + st * 16384, tb0 + st * 16384 + 8192); wo[2 * st + 1] = tr_pair(sm, tb1 + st * 16384, tb1 + st * 16384 + 8192); }
    SB();
  }
  BAR();
  TS(18);
  u32x4 dwf[2][5], wp[16], wb[16], wa[16], wa15;
  auto LA = [&](int q) { dma16s(rws, c.lane * 16, wt + WT_2A + (c.s * 8 + c.w) * 16384 + q * 1024, L_RING + c.w * 15360 + q * 1024); };
  auto LD = [&](int q) { SB(); dwf[q / 5][q % 5] = __builtin_amdgcn_raw_buffer_load_b128(rws, c.lane * 16 + (q & 3) * 1024, wt + WT_DW + c.w * 10240 + (q >> 2) * 4096, 16); SB(); };
  auto LP = [&](int q) { SB(); wp[q] = __builtin_amdgcn_raw_buffer_load_b128(rws, c.lane * 16 + (q & 3) * 1024, wt + WT_P2 + c.w * 16384 + (q >> 2) * 4096, 16); SB(); };
  auto LB = [&](int q) { SB(); wb[q] = __builtin_amdgcn_raw_buffer_load_b128(rws, c.lane * 16 + (q & 3) * 1024, wt + WT_2B + (c.s * 8 + c.w) * 16384 + (q >> 2) * 4096, 16); SB(); };
  {
    u32x4 at[2];
    auto lda = [&]() {
#pragma unroll
      for (int h = 0; h < 2; h++) at[h] = __builtin_amdgcn_raw_buffer_load_b128(rws, c.tid * 16 + h * 8192, WS_ATT + c.tg * 16384, 16); };
    DRAIN();
    __builtin_amdgcn_s_setprio(3); lda(); SB(); __builtin_amdgcn_s_setprio(0);
#pragma unroll
    for (int q = 3; q < 8; q++) w1[q] = __builtin_amdgcn_raw_buffer_load_b128(rws, c.lane * 16 + (q & 3) * 1024, wt + WT_P1 + (c.s * 8 + c.w) * 8192 + (q >> 2) * 4096, 16);
    SB();
    if (__any(max(umax4(at[0]), umax4(at[1])) == ~0u) && !dead) for (int sp = 0;; sp++) {
      asm volatile("" ::: "memory");
      lda();
      if (!__any(max(umax4(at[0]), umax4(at[1])) == ~0u)) break;
      if (sp > SPIN_CAP) { dead = true; break; }
    }
    lds_w128(sm, L_ACT1 + c.tid * 16, at[0]); lds_w128(sm, L_ACT1 + c.tid * 16 + 8192, at[1]);
    {
      const u32x4 ff = {~0u, ~0u, ~0u, ~0u};
      const unsigned o = c.w < 6 ? WS_P2 + (c.tg * 32 + c.s) * 4096 + ((c.s + 1 + (c.w >> 1)) & 3) * 16384 + (c.w & 1) * 2048 : 0x40000000u;
      st128(ff, rws, c.lane * 16, o); st128(ff, rws, c.lane * 16 + 1024, o);
    }
    TS(19);
    BAR();
    TS(20);
    zero_acc(acc);
    {
      u32x4 bq[16];
#pragma unroll
      for (int st = 0; st < 8; st++) { bq[2 * st] = lds_r128(sm, L_ACT1 + bbase + st * 2048); bq[2 * st + 1] = lds_r128(sm, L_ACT1 + bbase + st * 2048 + 256); }
#pragma unroll
      for (int st = 0; st < 8; st++) {
        LA(st);
#pragma unroll
        for (int i = 0; i < 2; i++) { acc[i][0] = mfma16(wo[st * 2 + i], bq[2 * st], acc[i][0]); acc[i][1] = mfma16(wo[st * 2 + i], bq[2 * st + 1], acc[i][1]); }
        SB();
      }
    }
#pragma unroll
    for (int th = 0; th < 2; th++) {
      float x[8], a[8]; unpack8(x, xr[th]); acc_row(a, acc, th);
#pragma unroll
      for (int e = 0; e < 8; e++) x[e] += a[e];
      xr[th] = pack8(x);
    }
    PIN(xr[0]); PIN(xr[1]); LA(8);
    ln_part1(xr, sm, L_STAT_B, c);
    LA(9);
    BAR(); LA(10); ln_part2(xr, sm, L_ACT0, L_STAT_B, c); LA(11);
    BAR();
    TS(21);
    f32x4 a2[2] = {f32x4{0.f, 0.f, 0.f, 0.f}, f32x4{0.f, 0.f, 0.f, 0.f}};
    const int base = L_ACT0 + (c.g * 32 + c.tok) * 16;
    u32x4 aq[16];
#pragma unroll
    for (int st = 0; st < 8; st++) { aq[2 * st] = lds_r128(sm, base + st * 2048); aq[2 * st + 1] = lds_r128(sm, base + st * 2048 + 256); }
    LA(12); LA(13); LA(14);
#pragma unroll
    for (int st = 0; st < 8; st++) { a2[0] = mfma16(aq[2 * st], w1[st], a2[0]); a2[1] = mfma16(aq[2 * st + 1], w1[st], a2[1]); if (st == 3) LD(0); }
    LD(1);
    const bool isA = c.tok < 8;
    const int th = c.tok >> 3;
    float gv[4];
#pragma unroll
    for (int j = 0; j < 4; j++) {
      float snd = isA ? a2[1][j] : a2[0][j];
      float rcv = __uint_as_float(__builtin_amdgcn_update_dpp(0, __float_as_uint(snd), 0x128, 0xf, 0xf, false));
      float a = isA ? a2[0][j] : rcv, b = isA ? rcv : a2[1][j];
      gv[j] = (c.tile * 32 + th * 16 + c.g * 4 + j < T_LEN) ? a * sigm_f(b) : 0.f;
    }
    VMCNT(5);
    __builtin_amdgcn_raw_buffer_store_b64(u32x2{san(pack2(gv[0], gv[1])), san(pack2(gv[2], gv[3]))}, rws, th * 8192 + (c.s * 64 + c.w * 8 + (c.tok & 7)) * 32 + c.g * 8, WS_G + (c.b * 64 + c.tile * 2) * 8192, 16);
  }
  BAR();
  TS(22);
  {
    LD(2); LD(3); LD(4); LD(5);
    u32x4 gi[4];
    auto ldg = [&]() {
#pragma unroll
      for (int q = 0; q < 4; q++) { const int ht = c.tile * 2 - 1 + q;
        gi[q] = __builtin_amdgcn_raw_buffer_load_b128(rws, (unsigned)ht < 64u ? WS_G + (c.b * 64 + ht) * 8192 + c.tid * 16 : 0x40000000, 0, 16); } };
    VMCNT(4);
    __builtin_amdgcn_s_setprio(3); ldg(); SB(); __builtin_amdgcn_s_setprio(0);
    LD(6); LD(7); LD(8); LD(9);
    if (__any(max(max(umax4(gi[0]), umax4(gi[1])), max(umax4(gi[2]), umax4(gi[3]))) == ~0u) && !dead) for (int sp = 0;; sp++) {
      asm volatile("" ::: "memory");
      ldg();
      if (!__any(max(max(umax4(gi[0]), umax4(gi[1])), max(umax4(gi[2]), umax4(gi[3]))) == ~0u)) break;
      if (sp > SPIN_CAP) { dead = true; break; }
    }
#pragma unroll
    for (int q = 0; q < 4; q++) lds_w128(sm, L_G + (c.tid >> 1) * 128 + (((q * 2 + (c.tid & 1)) ^ (c.tid & 4)) * 16), gi[q]);
    LP(0); LP(1); LP(2); LP(3);
    TS(23);
    BAR();
    TS(24);
    {
      const int n = c.lane & 3, sl0 = c.w * 32 + (c.lane >> 2);
      u32x4 in[2][5]; f32x4 d[2][2];
#pragma unroll
      for (int gq = 0; gq < 2; gq++)
#pragma unroll
        for (int m = 0; m < 5; m++) in[gq][m] = lds_r128(sm, L_G + (sl0 + gq * 16) * 128 + (((n + m) ^ ((sl0 & 2) << 1)) * 16));
      LP(4); LP(5);
      zero_acc(d);
#pragma unroll
      for (int j = 0; j < 9; j++) {
#pragma unroll
        for (int gq = 0; gq < 2; gq++) {
          u32x2 a = {dwf[gq][j >> 1][(j & 1) * 2], dwf[gq][j >> 1][(j & 1) * 2 + 1]};
          u32x2 b0 = {in[gq][j >> 1][(j & 1) * 2], in[gq][j >> 1][(j & 1) * 2 + 1]};
          u32x2 b1 = {in[gq][(j + 1) >> 1][((j + 1) & 1) * 2], in[gq][(j + 1) >> 1][((j + 1) & 1) * 2 + 1]};
          d[gq][0] = mfma4(a, b0, d[gq][0]); d[gq][1] = mfma4(a, b1, d[gq][1]);
        }
        if (j == 3 || j == 7) LP(6 + (j >> 2));
      }
#pragma unroll
      for (int gq = 0; gq < 2; gq++) {
        u32x4 o;
#pragma unroll
        for (int e = 0; e < 4; e++) { LP(8 + gq * 4 + e); unsigned u = silu2(d[gq][e >> 1][(e & 1) * 2], d[gq][e >> 1][(e & 1) * 2 + 1]); PIN(u); o[e] = u; }
        lds_w128(sm, L_G + (sl0 + gq * 16) * 128 + (sl0 & 2) * 32 + (n ^ (((sl0 >> 3) & 1) * 2)) * 16, o);
      }
    }
    BAR();
    TS(25);
    zero_acc(acc);
    {
      const int q = c.tok >> 2, pp = c.tok & 3;
      const int base = L_G + (c.g * 8 + q) * 128 + (q & 2) * 32 + pp * 8;
      u32x4 bq[16];
#pragma unroll
      for (int st = 0; st < 8; st++)
#pragma unroll
        for (int th = 0; th < 2; th++) { const int o = base + st * 4096 + ((th ^ (c.g & 1)) * 32); bq[2 * st + th] = tr_pair(sm, o, o + 512); }
      SB();
      wa15 = __builtin_amdgcn_raw_buffer_load_b128(rws, c.lane * 16, wt + WT_2A + (c.s * 8 + c.w) * 16384 + 15360, 16);
#pragma unroll
      for (int st = 0; st < 8; st++) {
        if (st & 1) LB(st / 2);
#pragma unroll
        for (int th = 0; th < 2; th++) { acc[0][th] = mfma16(wp[st * 2], bq[2 * st + th], acc[0][th]); acc[1][th] = mfma16(wp[st * 2 + 1], bq[2 * st + th], acc[1][th]); }
        SB();
        wa[2 * st] = lds_r128(sm, L_RING + c.w * 15360 + st * 2048 + c.lane * 16);
        if (st < 7) wa[2 * st + 1] = lds_r128(sm, L_RING + c.w * 15360 + st * 2048 + 1024 + c.lane * 16);
        SB();
      }
    }
#pragma unroll
    for (int th = 0; th < 2; th++) {
      float x[8], a[8]; unpack8(x, xr[th]); acc_row(a, acc, th);
#pragma unroll
      for (int e = 0; e < 8; e++) x[e] += a[e];
      xr[th] = pack8(x);
    }
    PIN(xr[0]); PIN(xr[1]); LB(4);
    ln_part1(xr, sm, L_STAT_B, c);
    LB(5);
    wa[15] = wa15;
    BAR(); LB(6); LB(7); ln_part2(xr, sm, L_ACT0, L_STAT_B, c); LB(8); LB(9); BAR();
    TS(26);
    zero_acc(acc);
    {
      u32x4 bq[16];
#pragma unroll
      for (int st = 0; st < 8; st++) { bq[2 * st] = lds_r128(sm, L_ACT0 + bbase + st * 2048); bq[2 * st + 1] = lds_r128(sm, L_ACT0 + bbase + st * 2048 + 256); }
      LB(10); LB(11);
#pragma unroll
      for (int st = 0; st < 8; st++) {
#pragma unroll
        for (int i = 0; i < 2; i++) { acc[i][0] = mfma16(wa[st * 2 + i], bq[2 * st], acc[i][0]); acc[i][1] = mfma16(wa[st * 2 + i], bq[2 * st + 1], acc[i][1]); }
        if (st & 1) LB(12 + st / 2);
      }
    }
#pragma unroll
    for (int th = 0; th < 2; th++) acc_row(v[th], acc, th);
    act_store(sm, L_ACT1, c, u32x4{silu2(v[0][0], v[0][1]), silu2(v[0][2], v[0][3]), silu2(v[0][4], v[0][5]), silu2(v[0][6], v[0][7])}, u32x4{silu2(v[1][0], v[1][1]), silu2(v[1][2], v[1][3]), silu2(v[1][4], v[1][5]), silu2(v[1][6], v[1][7])});
    BAR();
    TS(27);
    zero_acc(acc);
    u32x4 bq[16];
#pragma unroll
    for (int q = 0; q < 16; q++) bq[q] = lds_r128(sm, L_ACT1 + bbase + (q & 7) * 2048 + (q >> 3) * 256);
#pragma unroll
    for (int th = 0; th < 2; th++) {
#pragma unroll
      for (int st = 0; st < 8; st++) { acc[0][th] = mfma16(wb[st * 2], bq[th * 8 + st], acc[0][th]); acc[1][th] = mfma16(wb[st * 2 + 1], bq[th * 8 + st], acc[1][th]); }
      SB();
      acc_row(v[th], acc, th);
      const int dest = th * 2 + (c.tok >> 3);
      if (dest != c.s) {
        u32x4 o = pack8(v[th]); o = u32x4{san(o[0]), san(o[1]), san(o[2]), san(o[3])};
        st128(o, rws, dest * 16384 + ((c.w * 4 + c.g) * 8 + (c.tok & 7)) * 16, WS_P2 + (c.tg * 32 + c.s) * 4096);
      } else {
        float x[8]; unpack8(x, xr[th]);
        const int o = L_ACT0 + (c.tok & 7) * 1040 + (c.w * 32 + c.g * 4) * 4;
        *(f32x4*)(sm + o) = f32x4{fmaf(v[th][0], 0.5f, x[0]), fmaf(v[th][1], 0.5f, x[1]), fmaf(v[th][2], 0.5f, x[2]), fmaf(v[th][3], 0.5f, x[3])};
        *(f32x4*)(sm + o + 64) = f32x4{fmaf(v[th][4], 0.5f, x[4]), fmaf(v[th][5], 0.5f, x[5]), fmaf(v[th][6], 0.5f, x[6]), fmaf(v[th][7], 0.5f, x[7])};
      }
    }
  }
  TS(28);
  {
    const int tk = (c.lane >> 1) & 7, f0 = c.w * 32 + (c.lane & 1) * 16 + c.g * 4;
    asm volatile("s_waitcnt lgkmcnt(0)" ::: "memory");
    f32x4 x = *(f32x4*)(sm + L_ACT0 + tk * 1040 + f0 * 4);
    u32x2 pp[3];
    DRAIN();
    for (int sp = 0;; sp++) {
#pragma unroll
      for (int k = 0; k < 3; k++) pp[k] = __builtin_amdgcn_raw_buffer_load_b64(rws, c.tid * 8, WS_P2 + (c.tg * 32 + c.s * 4 + ((c.s + 1 + k) & 3)) * 4096, 16);
      unsigned mx = 0;
#pragma unroll
      for (int k = 0; k < 3; k++) mx = max(mx, max(pp[k][0], pp[k][1]));
      if (dead || !__any(mx == ~0u)) break;
      if (sp > SPIN_CAP) { dead = true; break; }
      asm volatile("" ::: "memory");
    }
    TS(29);
    f32x4 a = {0.f, 0.f, 0.f, 0.f};
#pragma unroll
    for (int k = 0; k < 3; k++) a += f32x4{lo16(pp[k][0]), hi16(pp[k][0]), lo16(pp[k][1]), hi16(pp[k][1])};
    x += 0.5f * a;
    float t = swadd32(x[0] + x[1] + x[2] + x[3], x[0] * x[0] + x[1] * x[1] + x[2] * x[2] + x[3] * x[3]);
    t = swadd16(t, t);
    t += __uint_as_float(__builtin_amdgcn_update_dpp(0, __float_as_uint(t), 0xB1, 0xf, 0xf, false));
    *(float*)(sm + L_STAT_B + c.tid * 4) = t;
    BAR();
    float u = 0.f;
#pragma unroll
    for (int k = 0; k < 8; k++) u += *(float*)(sm + L_STAT_B + (k * 64 + c.lane) * 4);
    auto r = __builtin_amdgcn_permlane32_swap(__float_as_uint(u), __float_as_uint(u), false, false);
    const float mean = __uint_as_float(r[0]) * (1.f / 256.f);
    const float rstd = __builtin_amdgcn_rsqf(fmaxf(fmaf(-mean, mean, __uint_as_float(r[1]) * (1.f / 256.f)), 0.f) + 1e-5f);
    x = (x - mean) * rstd;
    const int tt = c.tile * 32 + c.s * 8 + tk;
    __builtin_amdgcn_raw_buffer_store_b64(u32x2{pack2(x[0], x[1]), pack2(x[2], x[3])}, mk_rsrc(p.out, 2000 * 512), ((c.b * T_LEN + tt) * 512 + f0 * 2) | (tt < T_LEN ? 0 : 0x40000000), 0, OUT_AUX);
  }
  TS(30); VMW(); TS(31); TS_OUT(0);
}

extern "C" void kernel_launch(void* const* d_in, const int* in_sizes, int n_in, void* d_out, int out_size, void* d_ws, size_t ws_size, hipStream_t stream) {
  Params p;
  p.x = (const u16*)d_in[0]; p.w1a = (const u16*)d_in[1]; p.w1b = (const u16*)d_in[2]; p.wqkv = (const u16*)d_in[3]; p.wo = (const u16*)d_in[4];
  p.wp1 = (const u16*)d_in[5]; p.dw = (const u16*)d_in[6]; p.wp2 = (const u16*)d_in[7]; p.w2a = (const u16*)d_in[8]; p.w2b = (const u16*)d_in[9];
  p.out = (u16*)d_out; p.ws = (char*)d_ws;
  k_all<<<256, 512, 0, stream>>>(p);
}
```

```cpp
#include <hip/hip_runtime.h>
#include <hip/hip_bf16.h>

typedef unsigned short u16;
typedef unsigned long long u64;
typedef __attribute__((ext_vector_type(2))) unsigned u32x2;
typedef __attribute__((ext_vector_type(4))) unsigned u32x4;
typedef __attribute__((ext_vector_type(2))) float f32x2;
typedef __attribute__((ext_vector_type(4))) float f32x4;
typedef __attribute__((ext_vector_type(16))) float f32x16;
typedef __attribute__((ext_vector_type(2))) __bf16 bf16x2;
typedef __attribute__((ext_vector_type(4))) __bf16 bf16x4;
typedef __attribute__((ext_vector_type(8))) __bf16 bf16x8;
typedef __attribute__((ext_vector_type(4))) short s16x4;
typedef __amdgpu_buffer_rsrc_t rsrc_t;
#define DI __device__ __forceinline__
#define LAS __attribute__((address_space(3)))
#define SB() __builtin_amdgcn_sched_barrier(0)
#define PIN(x) asm volatile("" : "+v"(x))

constexpr int T_LEN = 1000;
constexpr float LOG2E = 1.4426950408889634f;

constexpr unsigned WS_WT  = 0;
constexpr unsigned WS_SYNC = 1536u << 10;
constexpr unsigned WS_KV  = 2u << 20;
constexpr unsigned WS_G   = 4u << 20;
constexpr unsigned WS_ATT = 5u << 20;
constexpr unsigned WS_P1  = 6u << 20;
constexpr unsigned WS_P2  = WS_P1 + 65536;
constexpr unsigned WT_P2 = 0, WT_2A = 128u << 10, WT_2B = 640u << 10, WT_P1 = 1152u << 10, WT_DW = 1408u << 10;
constexpr unsigned SY_E = 0, SY_F1 = 1024, SY_F2 = SY_F1 + 131072;
constexpr unsigned WS_END = 14u << 20;

constexpr int L_RING = 0, L_ACT0 = 131072, L_ACT1 = 147456, L_END = 163840;
constexpr int L_STAT_A = L_ACT1, L_Q = L_ACT1 + 4096, L_COMB = L_ACT0, L_ML = 122880, L_STAT_B = 124928, L_G = L_ACT0;
#define BAR() asm volatile("s_waitcnt lgkmcnt(0)\n\ts_barrier" ::: "memory")
#define VMCNT(n) asm volatile("s_waitcnt vmcnt(" #n ")" ::: "memory")
#define OUT_AUX 17
#define ROT 1

DI rsrc_t mk_rsrc(const void* p, unsigned bytes) { return __builtin_amdgcn_make_buffer_rsrc((void*)p, 0, bytes, 0x00020000); }
DI unsigned pack2(float a, float b) { bf16x2 v = {(__bf16)a, (__bf16)b}; return __builtin_bit_cast(unsigned, v); }
DI float lo16(unsigned u) { return __uint_as_float(u << 16); }
DI float hi16(unsigned u) { return __uint_as_float(u & 0xffff0000u); }
DI f32x4 mfma16(u32x4 a, u32x4 b, f32x4 c) { return __builtin_amdgcn_mfma_f32_16x16x32_bf16(__builtin_bit_cast(bf16x8, a), __builtin_bit_cast(bf16x8, b), c, 0, 0, 0); }
DI f32x16 mfma32(u32x4 a, u32x4 b, f32x16 c) { return __builtin_amdgcn_mfma_f32_32x32x16_bf16(__builtin_bit_cast(bf16x8, a), __builtin_bit_cast(bf16x8, b), c, 0, 0, 0); }
DI f32x4 mfma4(u32x2 a, u32x2 b, f32x4 c) { return __builtin_amdgcn_mfma_f32_4x4x4bf16_1k(__builtin_bit_cast(s16x4, a), __builtin_bit_cast(s16x4, b), c, 0, 0, 0); }
DI unsigned silu2(float a, float b) {
  const f32x2 x = {a, b}, t = x * f32x2{-LOG2E, -LOG2E}, d = f32x2{__builtin_amdgcn_exp2f(t[0]), __builtin_amdgcn_exp2f(t[1])} + f32x2{1.f, 1.f};
  const f32x2 y = x * f32x2{__builtin_amdgcn_rcpf(d[0]), __builtin_amdgcn_rcpf(d[1])};
  return pack2(y[0], y[1]);
}
DI float sigm_f(float x) { return __builtin_amdgcn_rcpf(1.f + __builtin_amdgcn_exp2f(-LOG2E * x)); }
DI u32x4 lds_r128(char* sm, int off) { return *(u32x4*)(sm + off); }
DI void lds_w128(char* sm, int off, u32x4 v) { *(u32x4*)(sm + off) = v; }
DI float swadd32(float a, float b) { auto r = __builtin_amdgcn_permlane32_swap(__float_as_uint(a), __float_as_uint(b), false, false); return __uint_as_float(r[0]) + __uint_as_float(r[1]); }
DI float swadd16(float a, float b) { auto r = __builtin_amdgcn_permlane16_swap(__float_as_uint(a), __float_as_uint(b), false, false); return __uint_as_float(r[0]) + __uint_as_float(r[1]); }

struct Ctx { int tid, w, lane, tok, g, b, tile, s, tg; };
DI Ctx mk_ctx() {
  Ctx c; c.tid = threadIdx.x; c.w = __builtin_amdgcn_readfirstlane(threadIdx.x >> 6); c.lane = c.tid & 63; c.tok = c.lane & 15; c.g = c.lane >> 4;
  int bid = blockIdx.x; c.s = (bid >> 3) & 3; c.tg = (bid & 7) * 8 + (bid >> 5); c.b = c.tg >> 5; c.tile = c.tg & 31; return c;
}

DI u32x4 tr_frag(const unsigned* r8, int i) {
  const unsigned sel = i ? 0x07060302u : 0x05040100u;
  u32x4 f;
#pragma unroll
  for (int m = 0; m < 4; m++) f[m] = __builtin_amdgcn_perm(r8[2 * m + 1], r8[2 * m], sel);
  return f;
}
DI void st128(u32x4 v, rsrc_t rs, unsigned voff, unsigned soff) { __builtin_amdgcn_raw_buffer_store_b128(v, rs, voff + soff, 0, 16); }
DI void dma16(rsrc_t rs, int voff, int soff, int lds) {
  asm volatile("s_mov_b32 m0, %0\n\ts_nop 0\n\tbuffer_load_dwordx4 %1, %2, %3 offen lds" :: "s"(lds), "v"(voff), "s"(rs), "s"(soff) : "memory");
}
DI void dma16s(rsrc_t rs, int voff, int soff, int lds) {
  asm volatile("s_mov_b32 m0, %0\n\ts_nop 0\n\tbuffer_load_dwordx4 %1, %2, %3 offen sc1 lds" :: "s"(lds), "v"(voff), "s"(rs), "s"(soff) : "memory");
}
DI unsigned touch(rsrc_t rs, unsigned voff, bool on = true) { unsigned r; voff |= on ? 0u : 0x40000000u; asm volatile("buffer_load_dword %0, %1, %2, 0 offen sc1" : "=&v"(r) : "v"(voff), "s"(rs) : "memory"); return r; }
DI u32x4 tr_pair(char* sm, int o0, int o1) {
  u32x2 lo = __builtin_bit_cast(u32x2, __builtin_amdgcn_ds_read_tr16_b64_v4bf16((LAS bf16x4*)(sm + o0)));
  u32x2 hi = __builtin_bit_cast(u32x2, __builtin_amdgcn_ds_read_tr16_b64_v4bf16((LAS bf16x4*)(sm + o1)));
  return u32x4{lo[0], lo[1], hi[0], hi[1]};
}
DI void gemm_tiled(f32x4 (&acc)[2][2], const u32x4 (&fr)[16], char* sm, int act, const Ctx& c) {
  const int base = act + (c.g * 32 + c.tok) * 16;
#pragma unroll
  for (int st = 0; st < 8; st++) {
    u32x4 b0 = lds_r128(sm, base + st * 2048), b1 = lds_r128(sm, base + st * 2048 + 256);
#pragma unroll
    for (int i = 0; i < 2; i++) {
      acc[i][0] = mfma16(fr[st * 2 + i], b0, acc[i][0]);
      acc[i][1] = mfma16(fr[st * 2 + i], b1, acc[i][1]);
    }
  }
}
DI void zero_acc(f32x4 (&acc)[2][2]) {
#pragma unroll
  for (int i = 0; i < 2; i++)
#pragma unroll
    for (int t = 0; t < 2; t++) acc[i][t] = f32x4{0.f, 0.f, 0.f, 0.f};
}
DI void unpack8(float (&v)[8], u32x4 p) {
#pragma unroll
  for (int m = 0; m < 4; m++) { v[2 * m] = lo16(p[m]); v[2 * m + 1] = hi16(p[m]); }
}
DI u32x4 pack8(const float (&v)[8]) { return u32x4{pack2(v[0], v[1]), pack2(v[2], v[3]), pack2(v[4], v[5]), pack2(v[6], v[7])}; }
DI void act_store(char* sm, int act, const Ctx& c, u32x4 p0, u32x4 p1) {
  const int o = act + ((c.w * 4 + c.g) * 32 + c.tok) * 16;
  lds_w128(sm, o, p0); lds_w128(sm, o + 256, p1);
}
DI void ln_part1(const u32x4 (&xr)[2], char* sm, int stat, const Ctx& c) {
  float s0 = 0, s1 = 0, q0 = 0, q1 = 0;
  unsigned ou = 0x3f803f80u; PIN(ou);
  const bf16x2 one = __builtin_bit_cast(bf16x2, ou);
#pragma unroll
  for (int m = 0; m < 4; m++) { const unsigned ua = xr[0][m], ub = xr[1][m]; const bf16x2 a = __builtin_bit_cast(bf16x2, ua), b = __builtin_bit_cast(bf16x2, ub);
    s0 = __builtin_amdgcn_fdot2_f32_bf16(a, one, s0, false); s1 = __builtin_amdgcn_fdot2_f32_bf16(b, one, s1, false);
    q0 = __builtin_amdgcn_fdot2_f32_bf16(a, a, q0, false); q1 = __builtin_amdgcn_fdot2_f32_bf16(b, b, q1, false); }
  float t = swadd16(swadd32(s0, s1), swadd32(q0, q1));
  *(float*)(sm + stat + (c.w * 64 + c.lane) * 4) = t;
}
DI void ln_part2(const u32x4 (&xr)[2], char* sm, int act, int stat, const Ctx& c) {
  float v[2][8]; unpack8(v[0], xr[0]); unpack8(v[1], xr[1]);
  float u = 0;
#pragma unroll
  for (int k = 0; k < 8; k++) u += *(float*)(sm + stat + (k * 64 + c.lane) * 4);
  auto r = __builtin_amdgcn_permlane16_swap(__float_as_uint(u), __float_as_uint(u), false, false);
  float mean = __uint_as_float(r[0]) * (1.f / 256.f);
  float var = fmaxf(fmaf(-mean, mean, __uint_as_float(r[1]) * (1.f / 256.f)), 0.f);
  float rstd = __builtin_amdgcn_rsqf(var + 1e-5f), nm = -mean * rstd;
  auto rr = __builtin_amdgcn_permlane32_swap(__float_as_uint(rstd), __float_as_uint(rstd), false, false);
  auto rn = __builtin_amdgcn_permlane32_swap(__float_as_uint(nm), __float_as_uint(nm), false, false);
#pragma unroll
  for (int t2 = 0; t2 < 2; t2++)
#pragma unroll
    for (int e = 0; e < 8; e += 2) { const float r_ = __uint_as_float(rr[t2]), n_ = __uint_as_float(rn[t2]);
      const f32x2 z = __builtin_elementwise_fma(f32x2{v[t2][e], v[t2][e + 1]}, f32x2{r_, r_}, f32x2{n_, n_}); v[t2][e] = z[0]; v[t2][e + 1] = z[1]; }
  act_store(sm, act, c, pack8(v[0]), pack8(v[1]));
}
DI void acc_row(float (&v)[8], const f32x4 (&acc)[2][2], int th) {
#pragma unroll
  for (int j = 0; j < 4; j++) { v[j] = acc[0][th][j]; v[4 + j] = acc[1][th][j]; }
}
struct Params { const u16 *x, *w1a, *w1b, *wqkv, *wo, *wp1, *dw, *wp2, *w2a, *w2b; u16* out; char* ws; };

DI unsigned ald16(rsrc_t rs, int voff) { unsigned r; asm volatile("buffer_load_ushort %0, %1, %2, 0 offen" : "=&v"(r) : "v"(voff), "s"(rs) : "memory"); return r; }
DI void prep_issue(const Params& p, int task, int lane, int lds, unsigned (&raw)[8]) {
  if (task < 352) {
    const int st = task & 7, r8 = lane >> 3, ck = lane & 7;
    const u16* src; int vo, so, step; unsigned bytes;
    if (task < 32) { src = p.wp2; bytes = 131072; vo = ((r8 >> 2) * 8 + (r8 & 3)) * 512 + ck * 16; so = st * 32 * 512 + (task >> 3) * 128; step = 0; }
    else if (task < 160) { src = p.w2a; bytes = 524288; vo = r8 * 2048 + ck * 16; so = st * 32 * 2048 + ((task - 32) >> 3) * 128; step = 8 * 2048; }
    else if (task < 288) { const int u = (task - 160) >> 3; src = p.w2b; bytes = 524288; vo = r8 * 512 + ck * 16; so = ((u >> 2) * 256 + st * 32) * 512 + (u & 3) * 128; step = 8 * 512; }
    else { src = p.wp1; bytes = 262144; vo = r8 * 1024 + (ck >> 1) * 16 + (ck & 1) * 512; so = st * 32 * 1024 + ((task - 288) >> 3) * 64; step = 8 * 1024; }
    rsrc_t rs = mk_rsrc(src, bytes);
#pragma unroll
    for (int i = 0; i < 4; i++) dma16(rs, vo + so + (task < 32 ? ((i & 1) * 16 + (i >> 1) * 4) * 512 : i * step), 0, lds + i * 1024);
  } else {
    const int t = task - 352, grp = t / 5, m = t - grp * 5;
    const int ch = (16 * grp + (lane >> 2)) & 255, i = lane & 3;
    rsrc_t rs = mk_rsrc(p.dw, 31 * 512);
#pragma unroll
    for (int e = 0; e < 8; e++) raw[e] = ald16(rs, (unsigned)(8 * m + e - 1 - i) < 31u ? (8 * m + e - 1 - i) * 512 + ch * 2 : 0x100000);
  }
}
DI void prep_finish(int task, int lane, char* sm, int lds, const unsigned (&raw)[8], rsrc_t rws) {
  if (task < 352) {
    const int st = task & 7, tb = lds + ((lane >> 4) * 4 + ((lane & 15) >> 2)) * 128 + (lane & 3) * 8;
    const unsigned d = task < 288 ? WS_WT + WT_P2 + (task >> 3) * 32768 + st * 2048 : WS_WT + WT_P1 + ((task - 288) >> 3) * 32768 + st * 1024;
#pragma unroll
    for (int i = 0; i < 4; i++) st128(tr_pair(sm, tb + i * 32, tb + i * 32 + 2048), rws, lane * 16, d + (task < 288 ? (i >> 1) * 16384 + (i & 1) * 1024 : i * 8192));
  } else st128(tr_frag(raw, 0), rws, lane * 16, task < 432 ? WS_WT + WT_DW + (task - 352) * 1024 : 0x40000000u);
}

#define KATTR __global__ __attribute__((amdgpu_waves_per_eu(2, 2))) __launch_bounds__(512, 2)

constexpr int PF_CODE = 27 * 1024 + 512;
constexpr int SPIN_CAP = 1 << 17;
#define DRAIN() asm volatile("s_waitcnt vmcnt(0)" ::: "memory")
DI u32x2 ld_flag(rsrc_t rws, int off) { return __builtin_amdgcn_raw_buffer_load_b64(rws, off, WS_SYNC, 16); }
DI void st_flag(rsrc_t rws, int off, u32x2 v) { __builtin_amdgcn_raw_buffer_store_b64(v, rws, off, WS_SYNC, 16); }
DI void poll_flags(rsrc_t rws, int off, u32x2 ep, bool& dead) {
  if (dead) return;
  for (int sp = 0;; sp++) {
    u32x2 f = ld_flag(rws, off);
    if (__all(f[0] == ep[0] && f[1] == ep[1])) break;
    if (sp > SPIN_CAP) { dead = true; break; }
    __builtin_amdgcn_s_sleep(2);
    asm volatile("" ::: "memory");
  }
  asm volatile("" ::: "memory");
}
DI void poll_flags(rsrc_t rws, int off, u32x2 ep, bool& dead, u32x2 first) { if (!__all(first[0] == ep[0] && first[1] == ep[1])) poll_flags(rws, off, ep, dead); asm volatile("" ::: "memory"); }
DI unsigned umax4(u32x4 v) { return max(max(v[0], v[1]), max(v[2], v[3])); }
DI unsigned san(unsigned v) { return min(v, 0xFFFFFFFEu); }

KATTR void k_all(Params p) {
  __shared__ __attribute__((aligned(16))) char sm[L_END];
  unsigned pt, pt0;
  const int jx = blockIdx.x >> 3;
  {
    const char* q = (const char*)(__builtin_amdgcn_s_getpc() & ~127ull) + min(jx * 8 + (int)(threadIdx.x >> 6), PF_CODE / 128 - 1) * 128;
    asm volatile("global_load_dword %0, %1, off" : "=&v"(pt) : "v"(q) : "memory");
  }
  const Ctx c = mk_ctx();
  rsrc_t rws = mk_rsrc(p.ws, WS_END);
  const unsigned wt = WS_WT;
  bool dead = false;
  u32x2 ep;
  u32x4 xr[2];
  f32x4 acc[2][2];
  float v[2][8];
  const int bbase = (c.g * 32 + c.tok) * 16;
    const int rot = ROT ? (c.tg & 3) : 0;
    rsrc_t rx = mk_rsrc(p.x, 2000 * 512), ra = mk_rsrc(p.w1a, 524288), rb = mk_rsrc(p.w1b, 524288), rq = mk_rsrc(p.wqkv, 393216), ro = mk_rsrc(p.wo, 131072);
#pragma unroll
    for (int i = 0; i < 2; i++) {
      const int row = c.w * 4 + i * 2 + (c.lane >> 5), t = c.tile * 32 + row;
      dma16(rx, ((c.b * T_LEN + t) * 512 + (((c.lane & 31) ^ (row & 15)) * 16)) | (t < T_LEN ? 0 : 0x40000000), 0, L_ACT0 + c.w * 2048 + i * 1024);
    }
    int va[4], vb[4];
#pragma unroll
    for (int i = 0; i < 4; i++) { const int row = 2 * i + (c.lane >> 5), ch = (c.lane & 31) ^ (row << 1); va[i] = row * 2048 + ch * 16; vb[i] = row * 512 + ch * 16; }
    const int vq = (c.lane >> 3) * 1536 + (((c.lane & 7) ^ (((c.lane >> 4) & 3) << 1)) * 16);
    auto dma_a = [&](int k) { const int kc = (k + rot) & 3;
#pragma unroll
      for (int i = 0; i < 4; i++) dma16(ra, va[i], (kc * 64 + c.w * 8) * 2048 + c.s * 512, L_RING + k * 32768 + c.w * 4096 + i * 1024); };
    auto dma_b = [&](int k) { const int kc = (k + rot) & 3;
#pragma unroll
      for (int i = 0; i < 4; i++) dma16(rb, vb[i], (c.s * 256 + kc * 64 + c.w * 8) * 512, L_RING + k * 32768 + c.w * 4096 + i * 1024); };
    auto dma_q = [&](int seg) {
#pragma unroll
      for (int i = 0; i < 4; i++) dma16(rq, vq, (c.w * 32 + i * 8) * 1536 + seg * 512 + c.s * 128, L_RING + seg * 32768 + c.w * 4096 + i * 1024); };
    const int trow = c.g * 4 + (c.tok >> 2), tp = c.tok & 3;
    const int tb0 = L_RING + trow * 512 + (((c.w * 4 + (tp >> 1)) ^ ((trow & 7) << 1)) * 16) + (tp & 1) * 8, tb1 = tb0 ^ 32;
    u32x4 fr[16];
    auto tr_chunk = [&](int k) {
#pragma unroll
      for (int h = 0; h < 2; h++) { const int o = k * 32768 + h * 16384;
        fr[(2 * k + h) * 2] = tr_pair(sm, tb0 + o, tb0 + o + 8192); fr[(2 * k + h) * 2 + 1] = tr_pair(sm, tb1 + o, tb1 + o + 8192); } };
    u32x4 bb[16];
    auto act_rd = [&](int k, int act) { const int o = act + bbase + ((k + rot) & 3) * 4096;
#pragma unroll
      for (int h = 0; h < 2; h++) { bb[k * 4 + h * 2] = lds_r128(sm, o + h * 2048); bb[k * 4 + h * 2 + 1] = lds_r128(sm, o + h * 2048 + 256); } };
    auto mm = [&](int q) {
#pragma unroll
      for (int i = 0; i < 2; i++) { acc[i][0] = mfma16(fr[q * 2 + i], bb[2 * q], acc[i][0]); acc[i][1] = mfma16(fr[q * 2 + i], bb[2 * q + 1], acc[i][1]); } };
    auto dma_b1 = [&](int k, int i) { dma16(rb, vb[i], (c.s * 256 + ((k + rot) & 3) * 64 + c.w * 8) * 512, L_RING + k * 32768 + c.w * 4096 + i * 1024); };
    auto dma_q1 = [&](int seg, int i) { dma16(rq, vq, (c.w * 32 + i * 8) * 1536 + seg * 512 + c.s * 128, L_RING + seg * 32768 + c.w * 4096 + i * 1024); };
    dma_a(0); dma_a(1); dma_a(2);
    asm volatile("s_waitcnt vmcnt(12)" : "+v"(pt) :: "memory");
    BAR();
#pragma unroll
    for (int th = 0; th < 2; th++) { const int o = L_ACT0 + (th * 16 + c.tok) * 512 + (c.g & 1) * 8, ck = c.w * 4 + (c.g >> 1);
      const u32x2 lo = *(u32x2*)(sm + o + ((ck ^ c.tok) * 16)), hi = *(u32x2*)(sm + o + (((ck + 2) ^ c.tok) * 16));
      xr[th] = u32x4{lo[0], lo[1], hi[0], hi[1]}; }
    dma_a(3);
    ln_part1(xr, sm, L_STAT_A, c);
    VMCNT(8); BAR();
    {
      const char* q = c.w == 0 ? (const char*)p.w1b + c.s * 131072 : c.w == 1 ? (const char*)p.w1b + c.s * 131072 + 65536 : c.w == 2 ? (const char*)p.wqkv : c.w == 3 ? (const char*)p.wo
        : c.w == 4 ? (const char*)p.out + (c.b * T_LEN + c.tile * 32) * 512 : p.ws + (c.w == 5 ? WS_P1 + c.tg * 131072 : c.w == 6 ? WS_KV : WS_G);
      asm volatile("global_load_dword %0, %1, off" : "=&v"(pt0) : "v"(q) : "memory");
    }
    asm volatile("s_nop 4\n\tbuffer_load_dwordx2 %0, %1, %2, 0 offen sc1" : "=&v"(ep) : "v"(WS_SYNC + SY_E), "s"(rws) : "memory");
    tr_chunk(0); tr_chunk(1);
    ln_part2(xr, sm, L_ACT0, L_STAT_A, c);
    VMCNT(2); BAR();
    act_rd(0, L_ACT0); act_rd(1, L_ACT0); SB(); tr_chunk(2); tr_chunk(3); act_rd(2, L_ACT0); act_rd(3, L_ACT0);
    zero_acc(acc);
#pragma unroll
    for (int q = 0; q < 4; q++) { dma_b1(0, q); mm(q); SB(); }
    BAR();
#pragma unroll
    for (int q = 0; q < 4; q++) { dma_b1(1, q); mm(4 + q); SB(); }
    u32x4 hp[2];
#pragma unroll
    for (int th = 0; th < 2; th++) { acc_row(v[th], acc, th);
#pragma unroll
      for (int e = 0; e < 4; e++) { dma_b1(2 + th, e); unsigned u = silu2(v[th][2 * e], v[th][2 * e + 1]); PIN(u); hp[th][e] = u; } }
    act_store(sm, L_ACT1, c, hp[0], hp[1]);
    {
      const int t = (c.w - 6) * 256 + blockIdx.x, r = (t & 7) * 32 + (c.lane & 31);
      const char* q = c.w < 6 ? (const char*)p.wqkv + ((jx >> 2) * 32 + (c.w & 1) * 16 + c.tok) * 1536 + (c.w >> 1) * 512 + c.s * 128
        : t < 32 ? (const char*)p.wp2 + r * 512 + (t >> 3) * 128 : t < 160 ? (const char*)p.w2a + r * 2048 + ((t - 32) >> 3) * 128 : t < 288 ? (const char*)p.w2b + ((((t - 160) >> 5) * 256 + r) * 512 + (((t - 160) >> 3) & 3) * 128)
        : t < 352 ? (const char*)p.wp1 + r * 1024 + ((t - 288) >> 3) * 64 + (c.lane >> 5) * 512 : (const char*)p.dw + min(c.lane & 31, 30) * 512 + ((t - 352) / 5 & 15) * 32;
      asm volatile("global_load_dword %0, %1, off" : "=&v"(pt) : "v"(q) : "memory");
    }
    asm volatile("s_waitcnt vmcnt(9)" : "+v"(ep), "+v"(pt0) :: "memory");
    BAR();
    { u64 e = (((u64)ep[1] << 32) | ep[0]) + 1; ep[0] = __builtin_amdgcn_readfirstlane((unsigned)e); ep[1] = __builtin_amdgcn_readfirstlane((unsigned)(e >> 32)); }
    tr_chunk(0); act_rd(0, L_ACT1); SB(); tr_chunk(1); act_rd(1, L_ACT1); SB(); act_rd(2, L_ACT1); act_rd(3, L_ACT1);
    zero_acc(acc);
    mm(0); mm(1); mm(2); mm(3); SB();
    VMCNT(1); BAR();
    tr_chunk(2); tr_chunk(3);
#pragma unroll
    for (int q = 0; q < 4; q++) { dma_q1(0, q); mm(4 + q); SB(); }
  u32x4 own[2];
#pragma unroll
  for (int th = 0; th < 2; th++) { acc_row(v[th], acc, th); own[th] = pack8(v[th]);
    st128(own[th], rws, c.tid * 16 + th * 8192, WS_P1 + (c.tg * 8 + c.s) * 16384); }
  dma_q(1);
  asm volatile("s_waitcnt vmcnt(4)" : "+v"(pt) :: "memory");
  st_flag(rws, c.lane ? 0x40000000 : SY_F1 + ((c.tg * 4 + c.s) * 8 + c.w) * 64, ep);
  BAR();
  dma_q(2);
  unsigned raw[8];
  const int task = (c.w - 6) * 256 + blockIdx.x, plds = L_RING + 3 * 32768 + (c.w & 1) * 4096;
  if (c.w >= 6) prep_issue(p, task, c.lane, plds, raw);
  else pt = touch(ro, (jx * 32 + c.lane) * 128, (c.tid >> 5) == 10);
  {
    poll_flags(rws, SY_F1 + ((c.tg * 4 + (c.lane & 3)) * 8 + c.w) * 64, ep, dead);
    u32x4 pp[3][2];
#pragma unroll
    for (int k = 0; k < 3; k++)
#pragma unroll
      for (int th = 0; th < 2; th++) asm volatile("buffer_load_dwordx4 %0, %1, %2, %3 offen sc1" : "=&v"(pp[k][th]) : "v"(c.tid * 16 + th * 8192), "s"(rws), "s"(WS_P1 + (c.tg * 8 + ((c.s + 1 + k) & 3)) * 16384) : "memory");
    BAR();
    u32x4 wq[16];
    {
      const int ws_ = c.w < 4 ? c.w + 2 : (c.w & 1);
      const int trow = c.g * 4 + (c.tok >> 2), tp = c.tok & 3;
      const int tb0 = L_RING + (ws_ >> 1) * 32768 + trow * 128 + ((((ws_ & 1) * 4 + (tp >> 1)) ^ (((trow >> 1) & 3) << 1)) * 16) + (tp & 1) * 8, tb1 = tb0 ^ 32;
#pragma unroll
      for (int st = 0; st < 8; st++) { wq[2 * st] = tr_pair(sm, tb0 + st * 4096, tb0 + st * 4096 + 2048); wq[2 * st + 1] = tr_pair(sm, tb1 + st * 4096, tb1 + st * 4096 + 2048); }
    }
    if (c.w >= 6) {
      asm volatile("" : "+v"(raw[0]), "+v"(raw[1]), "+v"(raw[2]), "+v"(raw[3]), "+v"(raw[4]), "+v"(raw[5]), "+v"(raw[6]), "+v"(raw[7]) :: "memory");
      prep_finish(task, c.lane, sm, plds, raw, rws);
      if (task < 352) VMCNT(4); else VMCNT(1);
    } else asm volatile("s_waitcnt vmcnt(0)" : "+v"(pt) :: "memory");
    asm volatile("" : "+v"(pp[0][0]), "+v"(pp[0][1]), "+v"(pp[1][0]), "+v"(pp[1][1]), "+v"(pp[2][0]), "+v"(pp[2][1]) :: "memory");
    SB();
#pragma unroll
    for (int th = 0; th < 2; th++) {
      float x[8], a[8], t[8]; unpack8(x, xr[th]); unpack8(a, own[th]);
#pragma unroll
      for (int k = 0; k < 3; k++) { unpack8(t, pp[k][th]);
#pragma unroll
        for (int e = 0; e < 8; e++) a[e] += t[e]; }
#pragma unroll
      for (int e = 0; e < 8; e++) x[e] = fmaf(a[e], 0.5f, x[e]);
      xr[th] = pack8(x);
    }
    ln_part1(xr, sm, L_STAT_A, c);
    {
      const u32x4 ff = {~0u, ~0u, ~0u, ~0u};
      st128(ff, rws, c.lane * 16, c.w < 4 ? WS_ATT + (c.tg * 4 + c.s) * 4096 + c.w * 1024 : WS_G + (c.b * 64 + c.tile * 2 + ((c.w >> 1) & 1)) * 8192 + c.s * 2048 + (c.w & 1) * 1024);
    }
    BAR();
    dma16(ro, ((c.w & 1) ? vb[2] : vb[0]), (28 + (c.w >> 1)) * 4096, L_RING + (28 + (c.w >> 1)) * 4096 + (c.w & 1) * 2048);
    dma16(ro, ((c.w & 1) ? vb[3] : vb[1]), (28 + (c.w >> 1)) * 4096, L_RING + (28 + (c.w >> 1)) * 4096 + (c.w & 1) * 2048 + 1024);
    ln_part2(xr, sm, L_ACT0, L_STAT_A, c); BAR();
    zero_acc(acc);
    const unsigned kvb = WS_KV + ((c.b * 4 + c.s) * 32 + c.tile) * 8192;
    if (c.w < 6 && (c.w & 2) == 0) {
      gemm_tiled(acc, wq, sm, L_ACT0, c);
#pragma unroll
      for (int th = 0; th < 2; th++) { acc_row(v[th], acc, th);
        const int o = (((c.w & 1) * 4 + c.g) * 32 + th * 16 + c.tok) * 16;
        if (c.w >= 4) lds_w128(sm, L_Q + o, pack8(v[th]));
        else st128(pack8(v[th]), rws, o, kvb); }
    } else if (c.w < 6) {
#pragma unroll
      for (int st = 0; st < 8; st++) {
        u32x4 b0 = lds_r128(sm, L_ACT0 + bbase + st * 2048), b1 = lds_r128(sm, L_ACT0 + bbase + st * 2048 + 256);
#pragma unroll
        for (int i = 0; i < 2; i++) { acc[i][0] = mfma16(b0, wq[st * 2 + i], acc[i][0]); acc[i][1] = mfma16(b1, wq[st * 2 + i], acc[i][1]); }
      }
#pragma unroll
      for (int th = 0; th < 2; th++) {
        auto r0 = __builtin_amdgcn_permlane32_swap(pack2(acc[0][th][0], acc[0][th][1]), pack2(acc[1][th][0], acc[1][th][1]), false, false);
        auto r1 = __builtin_amdgcn_permlane32_swap(pack2(acc[0][th][2], acc[0][th][3]), pack2(acc[1][th][2], acc[1][th][3]), false, false);
        u32x4 o = {r0[0], r1[0], r0[1], r1[1]};
        st128(o, rws, (8 * (c.tok >> 2) + 4 * (c.lane >> 5) + (c.tok & 3) + 32 * (c.g & 1)) * 16 + ((c.w & 1) * 2 + th) * 1024, kvb + 4096);
      }
    } else {
#pragma unroll 1
      for (int n = 0; n < 7; n++) { const int o = (n * 2 + (c.w & 1)) * 4096;
#pragma unroll
        for (int i = 0; i < 4; i++) dma16(ro, vb[i], o, L_RING + o + i * 1024); }
    }
  }
  if (c.w < 6) DRAIN(); else VMCNT(28);
  BAR();
  if (c.w == 0) st_flag(rws, c.lane ? 0x40000000 : SY_F2 + (c.tg * 4 + c.s) * 128, ep);
  u32x4 wo[16], w1[8];
  {
    u32x4 qf[4], kf[4][4], vf[4][4];
#pragma unroll
    for (int ks = 0; ks < 4; ks++) qf[ks] = lds_r128(sm, L_Q + c.lane * 16 + ks * 1024);
    wo[14] = tr_pair(sm, tb0 + 7 * 16384, tb0 + 7 * 16384 + 8192); wo[15] = tr_pair(sm, tb1 + 7 * 16384, tb1 + 7 * 16384 + 8192);
    BAR();
    if (c.w < 7) {
#pragma unroll
      for (int n = 0; n < 8; n++) dma16(ro, vb[n & 3], (14 + c.w * 2 + (n >> 2)) * 4096, L_RING + (14 + c.w * 2) * 4096 + n * 1024);
    }
    poll_flags(rws, SY_F2 + ((c.b * 32 + c.w * 4 + (c.lane & 3)) * 4 + c.s) * 128, ep, dead);
    const unsigned kvb = WS_KV + ((c.b * 4 + c.s) * 32 + c.w * 4) * 8192;
    __builtin_amdgcn_s_setprio(2);
#pragma unroll
    for (int t = 0; t < 4; t++)
#pragma unroll
      for (int ks = 0; ks < 4; ks++) kf[t][ks] = __builtin_amdgcn_raw_buffer_load_b128(rws, c.lane * 16 + ks * 1024, kvb + t * 8192, 16);
    SB();
    __builtin_amdgcn_s_setprio(1);
    f32x16 S[4];
    auto ldv = [&](int t) { SB();
#pragma unroll
      for (int ks = 0; ks < 4; ks++) vf[t][ks] = __builtin_amdgcn_raw_buffer_load_b128(rws, c.lane * 16 + ks * 1024, kvb + t * 8192 + 4096, 16);
      SB(); };
#pragma unroll
    for (int t = 0; t < 4; t++) {
#pragma unroll
      for (int e = 0; e < 16; e++) S[t][e] = 0.f;
#pragma unroll
      for (int ks = 0; ks < 4; ks++) S[t] = mfma32(kf[t][ks], qf[ks], S[t]);
    }
    ldv(0);
    __builtin_amdgcn_s_setprio(0);
    if (c.w == 7) {
#pragma unroll
      for (int e = 4; e < 16; e++) S[3][e] = -INFINITY;
    }
    float m = S[0][0];
#pragma unroll
    for (int t = 0; t < 4; t++)
#pragma unroll
      for (int e = 0; e < 16; e++) m = fmaxf(m, S[t][e]);
    { auto r = __builtin_amdgcn_permlane32_swap(__float_as_uint(m), __float_as_uint(m), false, false); m = fmaxf(__uint_as_float(r[0]), __uint_as_float(r[1])); }
    const float CS = 0.125f * LOG2E;
    float mc = -m * CS; PIN(mc);
    f32x2 l2 = {0.f, 0.f};
    f32x16 O[2];
#pragma unroll
    for (int e = 0; e < 16; e++) { O[0][e] = 0.f; O[1][e] = 0.f; }
    u32x4 pf[4][2]; u32x2 fl;
    auto pv = [&](int t) {
#pragma unroll
      for (int s2 = 0; s2 < 2; s2++) { O[0] = mfma32(vf[t][s2], pf[t][s2], O[0]); O[1] = mfma32(vf[t][2 + s2], pf[t][s2], O[1]); } };
#pragma unroll
    for (int t = 0; t < 4; t++) {
      if (t < 3) ldv(t + 1);
      else { SB();
        fl = ld_flag(rws, SY_F2 + ((((c.lane >> 4) & 1) * 32 + c.w * 4) * 4 + (c.lane & 15)) * 128);
        SB(); }
      if (t >= 2) pv(t - 2);
#pragma unroll
      for (int e = 0; e < 16; e += 2) {
        f32x2 z = __builtin_elementwise_fma(f32x2{S[t][e], S[t][e + 1]}, f32x2{CS, CS}, f32x2{mc, mc});
        z = f32x2{__builtin_amdgcn_exp2f(z[0]), __builtin_amdgcn_exp2f(z[1])}; l2 += z; pf[t][e >> 3][(e >> 1) & 3] = pack2(z[0], z[1]); }
      PIN(pf[t][0]); PIN(pf[t][1]); PIN(l2);
    }
    SB(); pv(2); pv(3);
    float l = l2[0] + l2[1]; l = swadd32(l, l);
    if (c.lane < 32) *(f32x2*)(sm + L_ML + (c.w * 32 + c.lane) * 8) = f32x2{m, l};
    const float rl = __builtin_amdgcn_rcpf(l);
#pragma unroll
    for (int dt = 0; dt < 2; dt++)
#pragma unroll
      for (int q = 0; q < 4; q++) *(u32x2*)(sm + L_COMB + ((c.w * 8 + dt * 4 + q) * 64 + c.lane) * 8) = u32x2{pack2(O[dt][4 * q] * rl, O[dt][4 * q + 1] * rl), pack2(O[dt][4 * q + 2] * rl, O[dt][4 * q + 3] * rl)};
    poll_flags(rws, SY_F2 + ((((c.lane >> 4) & 1) * 32 + c.w * 4) * 4 + (c.lane & 15)) * 128, ep, dead, fl);
    BAR();
    if (c.w == 0 && blockIdx.x == 0) st_flag(rws, c.lane ? 0x40000000 : SY_E, ep);
    f32x2 ml[8]; u32x2 ok[8]; float M = -INFINITY;
#pragma unroll
    for (int k = 0; k < 8; k++) ml[k] = *(f32x2*)(sm + L_ML + (k * 32 + (c.lane & 31)) * 8);
#pragma unroll
    for (int k = 0; k < 8; k++) ok[k] = *(u32x2*)(sm + L_COMB + ((k * 8 + c.w) * 64 + c.lane) * 8);
    SB();
#pragma unroll
    for (int q = 0; q < 3; q++) w1[q] = __builtin_amdgcn_raw_buffer_load_b128(rws, c.lane * 16 + (q & 3) * 1024, wt + WT_P1 + (c.s * 8 + c.w) * 8192 + (q >> 2) * 4096, 16);
    SB();
#pragma unroll
    for (int k = 0; k < 8; k++) M = fmaxf(M, ml[k][0]);
    float L = 0.f; f32x4 o = {0.f, 0.f, 0.f, 0.f};
#pragma unroll
    for (int k = 0; k < 8; k++) {
      const float f = __builtin_amdgcn_exp2f((ml[k][0] - M) * CS) * ml[k][1];
      L += f;
      o += f * f32x4{lo16(ok[k][0]), hi16(ok[k][0]), lo16(ok[k][1]), hi16(ok[k][1])};
    }
    o *= __builtin_amdgcn_rcpf(L);
    __builtin_amdgcn_raw_buffer_store_b64(u32x2{san(pack2(o[0], o[1])), san(pack2(o[2], o[3]))}, rws, (c.w * 32 + (c.lane & 31)) * 16 + (c.lane >> 5) * 8, WS_ATT + (c.tg * 4 + c.s) * 4096, 16);
    SB();
#pragma unroll
    for (int st = 0; st < 7; st++) { wo[2 * st] = tr_pair(sm, tb0 + st * 16384, tb0 + st * 16384 + 8192); wo[2 * st + 1] = tr_pair(sm, tb1 + st * 16384, tb1 + st * 16384 + 8192); }
    SB();
  }
  BAR();
  u32x4 dwf[2][5], wp[16], wb[16], wa[16], wa15;
  auto LA = [&](int q) { dma16s(rws, c.lane * 16, wt + WT_2A + (c.s * 8 + c.w) * 16384 + q * 1024, L_RING + c.w * 15360 + q * 1024); };
  auto LD = [&](int q) { SB(); dwf[q / 5][q % 5] = __builtin_amdgcn_raw_buffer_load_b128(rws, c.lane * 16 + (q & 3) * 1024, wt + WT_DW + c.w * 10240 + (q >> 2) * 4096, 16); SB(); };
  auto LP = [&](int q) { SB(); wp[q] = __builtin_amdgcn_raw_buffer_load_b128(rws, c.lane * 16 + (q & 3) * 1024, wt + WT_P2 + c.w * 16384 + (q >> 2) * 4096, 16); SB(); };
  auto LB = [&](int q) { SB(); wb[q] = __builtin_amdgcn_raw_buffer_load_b128(rws, c.lane * 16 + (q & 3) * 1024, wt + WT_2B + (c.s * 8 + c.w) * 16384 + (q >> 2) * 4096, 16); SB(); };
  {
    u32x4 at[2];
    auto lda = [&]() {
#pragma unroll
      for (int h = 0; h < 2; h++) at[h] = __builtin_amdgcn_raw_buffer_load_b128(rws, c.tid * 16 + h * 8192, WS_ATT + c.tg * 16384, 16); };
    DRAIN();
    __builtin_amdgcn_s_setprio(3); lda(); SB(); __builtin_amdgcn_s_setprio(0);
#pragma unroll
    for (int q = 3; q < 8; q++) w1[q] = __builtin_amdgcn_raw_buffer_load_b128(rws, c.lane * 16 + (q & 3) * 1024, wt + WT_P1 + (c.s * 8 + c.w) * 8192 + (q >> 2) * 4096, 16);
    SB();
    if (__any(max(umax4(at[0]), umax4(at[1])) == ~0u) && !dead) for (int sp = 0;; sp++) {
      asm volatile("" ::: "memory");
      lda();
      if (!__any(max(umax4(at[0]), umax4(at[1])) == ~0u)) break;
      if (sp > SPIN_CAP) { dead = true; break; }
    }
    lds_w128(sm, L_ACT1 + c.tid * 16, at[0]); lds_w128(sm, L_ACT1 + c.tid * 16 + 8192, at[1]);
    {
      const u32x4 ff = {~0u, ~0u, ~0u, ~0u};
      const unsigned o = c.w < 6 ? WS_P2 + (c.tg * 32 + c.s) * 4096 + ((c.s + 1 + (c.w >> 1)) & 3) * 16384 + (c.w & 1) * 2048 : 0x40000000u;
      st128(ff, rws, c.lane * 16, o); st128(ff, rws, c.lane * 16 + 1024, o);
    }
    BAR();
    zero_acc(acc);
    {
      u32x4 bq[16];
#pragma unroll
      for (int st = 0; st < 8; st++) { bq[2 * st] = lds_r128(sm, L_ACT1 + bbase + st * 2048); bq[2 * st + 1] = lds_r128(sm, L_ACT1 + bbase + st * 2048 + 256); }
#pragma unroll
      for (int st = 0; st < 8; st++) {
        LA(st);
#pragma unroll
        for (int i = 0; i < 2; i++) { acc[i][0] = mfma16(wo[st * 2 + i], bq[2 * st], acc[i][0]); acc[i][1] = mfma16(wo[st * 2 + i], bq[2 * st + 1], acc[i][1]); }
        SB();
      }
    }
#pragma unroll
    for (int th = 0; th < 2; th++) {
      float x[8], a[8]; unpack8(x, xr[th]); acc_row(a, acc, th);
#pragma unroll
      for (int e = 0; e < 8; e++) x[e] += a[e];
      xr[th] = pack8(x);
    }
    PIN(xr[0]); PIN(xr[1]); LA(8);
    ln_part1(xr, sm, L_STAT_B, c);
    LA(9);
    BAR(); LA(10); ln_part2(xr, sm, L_ACT0, L_STAT_B, c); LA(11);
    BAR();
    f32x4 a2[2] = {f32x4{0.f, 0.f, 0.f, 0.f}, f32x4{0.f, 0.f, 0.f, 0.f}};
    const int base = L_ACT0 + (c.g * 32 + c.tok) * 16;
    u32x4 aq[16];
#pragma unroll
    for (int st = 0; st < 8; st++) { aq[2 * st] = lds_r128(sm, base + st * 2048); aq[2 * st + 1] = lds_r128(sm, base + st * 2048 + 256); }
    LA(12); LA(13); LA(14);
#pragma unroll
    for (int st = 0; st < 8; st++) { a2[0] = mfma16(aq[2 * st], w1[st], a2[0]); a2[1] = mfma16(aq[2 * st + 1], w1[st], a2[1]); if (st == 3) LD(0); }
    LD(1);
    const bool isA = c.tok < 8;
    const int th = c.tok >> 3;
    float gv[4];
#pragma unroll
    for (int j = 0; j < 4; j++) {
      float snd = isA ? a2[1][j] : a2[0][j];
      float rcv = __uint_as_float(__builtin_amdgcn_update_dpp(0, __float_as_uint(snd), 0x128, 0xf, 0xf, false));
      float a = isA ? a2[0][j] : rcv, b = isA ? rcv : a2[1][j];
      gv[j] = (c.tile * 32 + th * 16 + c.g * 4 + j < T_LEN) ? a * sigm_f(b) : 0.f;
    }
    VMCNT(5);
    __builtin_amdgcn_raw_buffer_store_b64(u32x2{san(pack2(gv[0], gv[1])), san(pack2(gv[2], gv[3]))}, rws, th * 8192 + (c.s * 64 + c.w * 8 + (c.tok & 7)) * 32 + c.g * 8, WS_G + (c.b * 64 + c.tile * 2) * 8192, 16);
  }
  BAR();
  {
    LD(2); LD(3); LD(4); LD(5);
    u32x4 gi[4];
    auto ldg = [&]() {
#pragma unroll
      for (int q = 0; q < 4; q++) { const int ht = c.tile * 2 - 1 + q;
        gi[q] = __builtin_amdgcn_raw_buffer_load_b128(rws, (unsigned)ht < 64u ? WS_G + (c.b * 64 + ht) * 8192 + c.tid * 16 : 0x40000000, 0, 16); } };
    VMCNT(4);
    __builtin_amdgcn_s_setprio(3); ldg(); SB(); __builtin_amdgcn_s_setprio(0);
    LD(6); LD(7); LD(8); LD(9);
    if (__any(max(max(umax4(gi[0]), umax4(gi[1])), max(umax4(gi[2]), umax4(gi[3]))) == ~0u) && !dead) for (int sp = 0;; sp++) {
      asm volatile("" ::: "memory");
      ldg();
      if (!__any(max(max(umax4(gi[0]), umax4(gi[1])), max(umax4(gi[2]), umax4(gi[3]))) == ~0u)) break;
      if (sp > SPIN_CAP) { dead = true; break; }
    }
#pragma unroll
    for (int q = 0; q < 4; q++) lds_w128(sm, L_G + (c.tid >> 1) * 128 + (((q * 2 + (c.tid & 1)) ^ (c.tid & 4)) * 16), gi[q]);
    LP(0); LP(1); LP(2); LP(3);
    BAR();
    {
      const int n = c.lane & 3, sl0 = c.w * 32 + (c.lane >> 2);
      u32x4 in[2][5]; f32x4 d[2][2];
#pragma unroll
      for (int gq = 0; gq < 2; gq++)
#pragma unroll
        for (int m = 0; m < 5; m++) in[gq][m] = lds_r128(sm, L_G + (sl0 + gq * 16) * 128 + (((n + m) ^ ((sl0 & 2) << 1)) * 16));
      LP(4); LP(5);
      zero_acc(d);
#pragma unroll
      for (int j = 0; j < 9; j++) {
#pragma unroll
        for (int gq = 0; gq < 2; gq++) {
          u32x2 a = {dwf[gq][j >> 1][(j & 1) * 2], dwf[gq][j >> 1][(j & 1) * 2 + 1]};
          u32x2 b0 = {in[gq][j >> 1][(j & 1) * 2], in[gq][j >> 1][(j & 1) * 2 + 1]};
          u32x2 b1 = {in[gq][(j + 1) >> 1][((j + 1) & 1) * 2], in[gq][(j + 1) >> 1][((j + 1) & 1) * 2 + 1]};
          d[gq][0] = mfma4(a, b0, d[gq][0]); d[gq][1] = mfma4(a, b1, d[gq][1]);
        }
        if (j == 3 || j == 7) LP(6 + (j >> 2));
      }
#pragma unroll
      for (int gq = 0; gq < 2; gq++) {
        u32x4 o;
#pragma unroll
        for (int e = 0; e < 4; e++) { LP(8 + gq * 4 + e); unsigned u = silu2(d[gq][e >> 1][(e & 1) * 2], d[gq][e >> 1][(e & 1) * 2 + 1]); PIN(u); o[e] = u; }
        lds_w128(sm, L_G + (sl0 + gq * 16) * 128 + (sl0 & 2) * 32 + (n ^ (((sl0 >> 3) & 1) * 2)) * 16, o);
      }
    }
    BAR();
    zero_acc(acc);
    {
      const int q = c.tok >> 2, pp = c.tok & 3;
      const int base = L_G + (c.g * 8 + q) * 128 + (q & 2) * 32 + pp * 8;
      u32x4 bq[16];
#pragma unroll
      for (int st = 0; st < 8; st++)
#pragma unroll
        for (int th = 0; th < 2; th++) { const int o = base + st * 4096 + ((th ^ (c.g & 1)) * 32); bq[2 * st + th] = tr_pair(sm, o, o + 512); }
      SB();
      wa15 = __builtin_amdgcn_raw_buffer_load_b128(rws, c.lane * 16, wt + WT_2A + (c.s * 8 + c.w) * 16384 + 15360, 16);
#pragma unroll
      for (int st = 0; st < 8; st++) {
        if (st & 1) LB(st / 2);
#pragma unroll
        for (int th = 0; th < 2; th++) { acc[0][th] = mfma16(wp[st * 2], bq[2 * st + th], acc[0][th]); acc[1][th] = mfma16(wp[st * 2 + 1], bq[2 * st + th], acc[1][th]); }
        SB();
        wa[2 * st] = lds_r128(sm, L_RING + c.w * 15360 + st * 2048 + c.lane * 16);
        if (st < 7) wa[2 * st + 1] = lds_r128(sm, L_RING + c.w * 15360 + st * 2048 + 1024 + c.lane * 16);
        SB();
      }
    }
#pragma unroll
    for (int th = 0; th < 2; th++) {
      float x[8], a[8]; unpack8(x, xr[th]); acc_row(a, acc, th);
#pragma unroll
      for (int e = 0; e < 8; e++) x[e] += a[e];
      xr[th] = pack8(x);
    }
    PIN(xr[0]); PIN(xr[1]); LB(4);
    ln_part1(xr, sm, L_STAT_B, c);
    LB(5);
    wa[15] = wa15;
    BAR(); LB(6); LB(7); ln_part2(xr, sm, L_ACT0, L_STAT_B, c); LB(8); LB(9); BAR();
    zero_acc(acc);
    {
      u32x4 bq[16];
#pragma unroll
      for (int st = 0; st < 8; st++) { bq[2 * st] = lds_r128(sm, L_ACT0 + bbase + st * 2048); bq[2 * st + 1] = lds_r128(sm, L_ACT0 + bbase + st * 2048 + 256); }
      LB(10); LB(11);
#pragma unroll
      for (int st = 0; st < 8; st++) {
#pragma unroll
        for (int i = 0; i < 2; i++) { acc[i][0] = mfma16(wa[st * 2 + i], bq[2 * st], acc[i][0]); acc[i][1] = mfma16(wa[st * 2 + i], bq[2 * st + 1], acc[i][1]); }
        if (st & 1) LB(12 + st / 2);
      }
    }
#pragma unroll
    for (int th = 0; th < 2; th++) acc_row(v[th], acc, th);
    act_store(sm, L_ACT1, c, u32x4{silu2(v[0][0], v[0][1]), silu2(v[0][2], v[0][3]), silu2(v[0][4], v[0][5]), silu2(v[0][6], v[0][7])}, u32x4{silu2(v[1][0], v[1][1]), silu2(v[1][2], v[1][3]), silu2(v[1][4], v[1][5]), silu2(v[1][6], v[1][7])});
    BAR();
    zero_acc(acc);
    u32x4 bq[16];
#pragma unroll
    for (int q = 0; q < 16; q++) bq[q] = lds_r128(sm, L_ACT1 + bbase + (q & 7) * 2048 + (q >> 3) * 256);
#pragma unroll
    for (int th = 0; th < 2; th++) {
#pragma unroll
      for (int st = 0; st < 8; st++) { acc[0][th] = mfma16(wb[st * 2], bq[th * 8 + st], acc[0][th]); acc[1][th] = mfma16(wb[st * 2 + 1], bq[th * 8 + st], acc[1][th]); }
      SB();
      acc_row(v[th], acc, th);
      const int dest = th * 2 + (c.tok >> 3);
      if (dest != c.s) {
        u32x4 o = pack8(v[th]); o = u32x4{san(o[0]), san(o[1]), san(o[2]), san(o[3])};
        st128(o, rws, dest * 16384 + ((c.w * 4 + c.g) * 8 + (c.tok & 7)) * 16, WS_P2 + (c.tg * 32 + c.s) * 4096);
      } else {
        float x[8]; unpack8(x, xr[th]);
        const int o = L_ACT0 + (c.tok & 7) * 1040 + (c.w * 32 + c.g * 4) * 4;
        *(f32x4*)(sm + o) = f32x4{fmaf(v[th][0], 0.5f, x[0]), fmaf(v[th][1], 0.5f, x[1]), fmaf(v[th][2], 0.5f, x[2]), fmaf(v[th][3], 0.5f, x[3])};
        *(f32x4*)(sm + o + 64) = f32x4{fmaf(v[th][4], 0.5f, x[4]), fmaf(v[th][5], 0.5f, x[5]), fmaf(v[th][6], 0.5f, x[6]), fmaf(v[th][7], 0.5f, x[7])};
      }
    }
  }
  {
    const int tk = (c.lane >> 1) & 7, f0 = c.w * 32 + (c.lane & 1) * 16 + c.g * 4;
    asm volatile("s_waitcnt lgkmcnt(0)" ::: "memory");
    f32x4 x = *(f32x4*)(sm + L_ACT0 + tk * 1040 + f0 * 4);
    u32x2 pp[3];
    DRAIN();
    for (int sp = 0;; sp++) {
#pragma unroll
      for (int k = 0; k < 3; k++) pp[k] = __builtin_amdgcn_raw_buffer_load_b64(rws, c.tid * 8, WS_P2 + (c.tg * 32 + c.s * 4 + ((c.s + 1 + k) & 3)) * 4096, 16);
      unsigned mx = 0;
#pragma unroll
      for (int k = 0; k < 3; k++) mx = max(mx, max(pp[k][0], pp[k][1]));
      if (dead || !__any(mx == ~0u)) break;
      if (sp > SPIN_CAP) { dead = true; break; }
      asm volatile("" ::: "memory");
    }
    f32x4 a = {0.f, 0.f, 0.f, 0.f};
#pragma unroll
    for (int k = 0; k < 3; k++) a += f32x4{lo16(pp[k][0]), hi16(pp[k][0]), lo16(pp[k][1]), hi16(pp[k][1])};
    x += 0.5f * a;
    float t = swadd32(x[0] + x[1] + x[2] + x[3], x[0] * x[0] + x[1] * x[1] + x[2] * x[2] + x[3] * x[3]);
    t = swadd16(t, t);
    t += __uint_as_float(__builtin_amdgcn_update_dpp(0, __float_as_uint(t), 0xB1, 0xf, 0xf, false));
    *(float*)(sm + L_STAT_B + c.tid * 4) = t;
    BAR();
    float u = 0.f;
#pragma unroll
    for (int k = 0; k < 8; k++) u += *(float*)(sm + L_STAT_B + (k * 64 + c.lane) * 4);
    auto r = __builtin_amdgcn_permlane32_swap(__float_as_uint(u), __float_as_uint(u), false, false);
    const float mean = __uint_as_float(r[0]) * (1.f / 256.f);
    const float rstd = __builtin_amdgcn_rsqf(fmaxf(fmaf(-mean, mean, __uint_as_float(r[1]) * (1.f / 256.f)), 0.f) + 1e-5f);
    x = (x - mean) * rstd;
    const int tt = c.tile * 32 + c.s * 8 + tk;
    __builtin_amdgcn_raw_buffer_store_b64(u32x2{pack2(x[0], x[1]), pack2(x[2], x[3])}, mk_rsrc(p.out, 2000 * 512), ((c.b * T_LEN + tt) * 512 + f0 * 2) | (tt < T_LEN ? 0 : 0x40000000), 0, OUT_AUX);
  }
}

extern "C" void kernel_launch(void* const* d_in, const int* in_sizes, int n_in, void* d_out, int out_size, void* d_ws, size_t ws_size, hipStream_t stream) {
  Params p;
  p.x = (const u16*)d_in[0]; p.w1a = (const u16*)d_in[1]; p.w1b = (const u16*)d_in[2]; p.wqkv = (const u16*)d_in[3]; p.wo = (const u16*)d_in[4];
  p.wp1 = (const u16*)d_in[5]; p.dw = (const u16*)d_in[6]; p.wp2 = (const u16*)d_in[7]; p.w2a = (const u16*)d_in[8]; p.w2b = (const u16*)d_in[9];
  p.out = (u16*)d_out; p.ws = (char*)d_ws;
  k_all<<<256, 512, 0, stream>>>(p);
}
```
